```python
import torch

def _softmax(x, axis=-1):
    return torch.softmax(x, dim=axis)
def _gelu(x):
    return torch.nn.functional.gelu(x, approximate="tanh")
def _silu(x):
    return torch.nn.functional.silu(x)
def _sigmoid(x):
    return torch.sigmoid(x)
def _layernorm(x, axis=-1, eps=1e-5):
    m = x.mean(dim=axis, keepdim=True)
    v = x.var(dim=axis, keepdim=True, unbiased=False)
    return (x - m) / torch.sqrt(v + eps)
def _scatter_add(tgt, idx, src, axis=0):
    return tgt.clone().index_add_(axis, idx, src)
def _segment_sum(src, seg, n):
    out = torch.zeros((n,) + src.shape[1:], dtype=src.dtype)
    return out.index_add_(0, seg, src)
def _pad(x, pw):
    flat = tuple(v for pair in reversed(pw) for v in pair)
    return torch.nn.functional.pad(x, flat)

kernel_name = 'gated_mix__grouped_kv_mqa__glu_gated_geglu__prompt'

def setup_inputs():

    _w = (lambda a: (lambda t: t.to(torch.bfloat16) if t.is_floating_point() else t)(torch.as_tensor(a)))
    def _A_setup():
        B, S, D, H = 1, 2048, 640, 32
        Dh = D // H
        return {
            "x": _w((lambda *s: torch.randn(*s, dtype=torch.bfloat16) * 0.05)(B, S, D)),
            "wq": _w((lambda *s: torch.randn(*s, dtype=torch.bfloat16) * 0.8)(D, H * Dh)),
            "wk": _w((lambda *s: torch.randn(*s, dtype=torch.bfloat16) * 0.8)(D, Dh)),
            "wv": _w((lambda *s: torch.randn(*s, dtype=torch.bfloat16) * 0.05)(D, Dh)),
            "wo": _w((lambda *s: torch.randn(*s, dtype=torch.bfloat16) * 0.05)(H * Dh, D)),
        }
    _A_in = _A_setup()

    def _B_setup():
        B, S, Dm, Df = 1, 2048, 640, 1536
        return {
            "x": _w((lambda *s: torch.randn(*s, dtype=torch.bfloat16) * 0.05)(B, S, Dm)),
            "w_gate": _w((lambda *s: torch.randn(*s, dtype=torch.bfloat16) * 0.8)(Dm, Df) * (Dm ** -0.5)),
            "w_up": _w((lambda *s: torch.randn(*s, dtype=torch.bfloat16) * 0.8)(Dm, Df) * (Dm ** -0.5)),
            "w_down": _w((lambda *s: torch.randn(*s, dtype=torch.bfloat16) * 0.8)(Df, Dm) * (Df ** -0.5)),
        }
    _B_in = _B_setup()

    return {
        "A_x": _A_in["x"],
        "A_wq": _A_in["wq"],
        "A_wk": _A_in["wk"],
        "A_wv": _A_in["wv"],
        "A_wo": _A_in["wo"],
        "B_x": _B_in["x"],
        "B_w_gate": _B_in["w_gate"],
        "B_w_up": _B_in["w_up"],
        "B_w_down": _B_in["w_down"],
        "gate_w": _w((lambda *s: torch.randn(*s, dtype=torch.bfloat16) * 0.05)(640) * 16.0),
    }

def reference(A_x, A_wq, A_wk, A_wv, A_wo, B_x, B_w_gate, B_w_up, B_w_down, gate_w):
    def _A_ref(x, wq, wk, wv, wo):
        B, S, D = x.shape
        H = 32
        Dh = D // H
        q = (lambda x, *d: x.permute(*d))((x @ wq).reshape(B, S, H, Dh), 0, 2, 1, 3)
        k = x @ wk
        v = x @ wv
        scores = torch.einsum("bhqd,bkd->bhqk", q, k) * (Dh ** -0.5)
        p = _softmax(scores, axis=-1)
        o = torch.einsum("bhqk,bkd->bhqd", p, v)
        return (lambda x, *d: x.permute(*d))(o, 0, 2, 1, 3).reshape(B, S, H * Dh) @ wo

    def _B_ref(x, w_gate, w_up, w_down):
        g = torch.einsum("bsd,df->bsf", x, w_gate)
        u = torch.einsum("bsd,df->bsf", x, w_up)
        h = _gelu(g) * u
        return torch.einsum("bsf,fd->bsd", h, w_down)

    x = A_x
    g = _sigmoid((x * gate_w).sum(-1, keepdims=True))
    a = _A_ref(x, A_wq, A_wk, A_wv, A_wo)
    b = _B_ref(x, B_w_gate, B_w_up, B_w_down)
    return g * a + (1 - g) * b
```

```cpp
#include <hip/hip_runtime.h>
#include <hip/hip_bf16.h>
#include <stdint.h>

#define STAMPS 0

typedef __attribute__((ext_vector_type(8))) short bf16x8;
typedef __attribute__((ext_vector_type(4))) short s16x4;
typedef __attribute__((ext_vector_type(2))) __bf16 bf16x2_t;
typedef __attribute__((ext_vector_type(16))) float f32x16;
typedef __attribute__((ext_vector_type(4))) float f32x4;
typedef __attribute__((ext_vector_type(4))) unsigned u32x4;
typedef __attribute__((ext_vector_type(2))) unsigned u32x2;
#define LAS __attribute__((address_space(3)))
#define GAS __attribute__((address_space(1)))
typedef LAS char* lptr;

constexpr int SEQ = 2048, DM = 640, DFF = 1536;
constexpr size_t WS_EPOCH = 0, WS_KVF = 1024, WS_HF = 4096, WS_OF = 8192, WS_TMO = 20480, WS_STAMP = 16u  __H0__ ,
                 WS_KV = 131072, WS_H = 1u << 20, WS_O = 8u << 20;
constexpr int UP_STAGE = 40992;
constexpr int L_G = 0;
constexpr int L_Q = UP_STAGE;
constexpr int Q_STAGE = 18432;
constexpr int L_MISC = 3 * UP_STAGE;
constexpr int L_CTRL = L_MISC;
constexpr int L_OMG = L_MISC + 128;
constexpr int L_GW = L_MISC + 384;
constexpr int L_ONES = L_MISC + 1664;
constexpr int L_KV = L_MISC + 4000 + 128;
constexpr int KV_CHUNK = 10240;
constexpr int LDS_BYTES = L_KV + 3 * KV_CHUNK;
static_assert(L_KV % 16 == 0 && L_ONES % 16 == 0 && L_GW % 16 == 0 && L_ONES + 3 * 640 + 16 <= L_KV, "lds map");
static_assert(LDS_BYTES <= 163840, "lds size");
constexpr int DN_SLOT = 9216, DN_WAVE = 3 * DN_SLOT;
enum { C_GW = 0, C_Q, C_QDONE, C_KVRDY, C_FULL, C_FREE, C_G, C_X, C_TAG, C_KMAX, C_OMG };

#define MFMA32(a, b, c) __builtin_amdgcn_mfma_f32_32x32x16_bf16(a, b, c, 0, 0, 0)
#define MFMA16(a, b, c) __builtin_amdgcn_mfma_f32_16x16x32_bf16(a, b, c, 0, 0, 0)
#define SBAR() __builtin_amdgcn_sched_barrier(0)
#define PIN(x) asm volatile("" : "+v"(x))
#define VMCNT(n) asm volatile("s_waitcnt vmcnt(" #n ")" ::: "memory")
#define LGKM0() asm volatile("s_waitcnt lgkmcnt(0)" ::: "memory")

struct Params {
  const char *x, *wq, *wk, *wv, *wo, *wg, *wu, *wd, *gw;
  char* out;
  char* ws;
};

__device__ __forceinline__ unsigned cvtpk(float lo, float hi) {
  typedef __attribute__((ext_vector_type(2))) float f32x2;
  bf16x2_t r = __builtin_convertvector((f32x2){lo, hi}, bf16x2_t);
  return __builtin_bit_cast(unsigned, r);
}
__device__ __forceinline__ float bflo(unsigned u) { return __builtin_bit_cast(float, u << 16); }
__device__ __forceinline__ float bfhi(unsigned u) { return __builtin_bit_cast(float, u & 0xffff0000u); }
__device__ __forceinline__ void dma16(unsigned voff, const char* base, unsigned lds) {
  asm volatile("s_mov_b32 m0, %2\n\ts_nop 0\n\tglobal_load_lds_dwordx4 %0, %1" ::"v"(voff), "s"(base), "s"(lds) : "memory");
}
__device__ __forceinline__ void dma16_dev(unsigned voff, const char* base, unsigned lds) {
  asm volatile("s_mov_b32 m0, %2\n\ts_nop 0\n\tglobal_load_lds_dwordx4 %0, %1 sc1" ::"v"(voff), "s"(base), "s"(lds) : "memory");
}
__device__ __forceinline__ void st16_dev(char* base, unsigned voff, u32x4 v) {
  asm volatile("global_store_dwordx4 %0, %1, %2 sc1\n\ts_nop 1" ::"v"(voff), "v"(v), "s"(base) : "memory");
}
__device__ __forceinline__ void st8_dev(char* base, unsigned voff, u32x2 v) {
  asm volatile("global_store_dwordx2 %0, %1, %2 sc1" ::"v"(voff), "v"(v), "s"(base) : "memory");
}
__device__ __forceinline__ void st4_dev(char* base, unsigned voff, unsigned v) {
  asm volatile("global_store_dword %0, %1, %2 sc1" ::"v"(voff), "v"(v), "s"(base) : "memory");
}
__device__ __forceinline__ bf16x8 ld128(lptr p) { return *(LAS bf16x8*)p; }
__device__ __forceinline__ s16x4 ldtr(lptr p) {
  typedef short v4i16_t __attribute__((ext_vector_type(4)));
  return __builtin_amdgcn_ds_read_tr16_b64_v4i16((LAS v4i16_t*)p);
}
__device__ __forceinline__ bf16x8 ldtr2(lptr p, int d) {
  s16x4 a = ldtr(p), b = ldtr(p + d);
  return __builtin_shufflevector(a, b, 0, 1, 2, 3, 4, 5, 6, 7);
}
__device__ __forceinline__ void arrive(lptr lds, int c) {
  unsigned a = (unsigned)(uintptr_t)(lds + L_CTRL + 4 * c);
  asm volatile("s_mov_b64 exec, 1\n\tds_add_u32 %0, %1\n\ts_mov_b64 exec, -1" ::"v"(a), "v"(1u) : "memory");
}
constexpr unsigned SPIN_LDS = 1u << 20, SPIN_GLB = 1u << 17;
__device__ __forceinline__ unsigned ldctl(lptr lds, int c) {
  return __builtin_amdgcn_readfirstlane(*(volatile LAS unsigned*)(lds + L_CTRL + 4 * c));
}
__device__ __forceinline__ void wait_ge(lptr lds, int c, unsigned target) {
  unsigned a = (unsigned)(uintptr_t)(lds + L_CTRL + 4 * c), v, sv, cnt;
  asm volatile(
      "s_mov_b32 %2, %5\n"
      ".Lw%=:\n\t"
      "ds_read_b32 %0, %3\n\t"
      "s_waitcnt lgkmcnt(0)\n\t"
      "v_readfirstlane_b32 %1, %0\n\t"
      "s_cmp_ge_u32 %1, %4\n\t"
      "s_cbranch_scc1 .Lx%=\n\t"
      "s_sleep 1\n\t"
      "s_sub_u32 %2, %2, 1\n\t"
      "s_cbranch_scc0 .Lw%=\n"
      ".Lx%=:"
      : "=&v"(v), "=&s"(sv), "=&s"(cnt)
      : "v"(a), "s"(target), "i"(SPIN_LDS)
      : "memory", "scc");
}
__device__ __forceinline__ float swap_add(float v) {
  auto r = __builtin_amdgcn_permlane32_swap(__float_as_uint(v), __float_as_uint(v), false, false);
  return __uint_as_float(r[0]) + __uint_as_float(r[1]);
}
__device__ __forceinline__ float swap_max(float v) {
  auto r = __builtin_amdgcn_permlane32_swap(__float_as_uint(v), __float_as_uint(v), false, false);
  return __builtin_fmaxf(__uint_as_float(r[0]), __uint_as_float(r[1]));
}
__device__ __forceinline__ float upper_half(float v) {
  auto r = __builtin_amdgcn_permlane32_swap(__float_as_uint(v), __float_as_uint(v), false, false);
  return __uint_as_float(r[1]);
}
#define STAMP(k) do {} while (0)

__device__ __forceinline__ void role_a(const Params& p, lptr lds, const int wave, const int lane, const int ti, const int tj) {
  const int tb = wave & 1, hp = (wave >> 1) & 1, kh = wave >> 2;
  const int r32 = lane & 31, hh = lane >> 5;
  const unsigned lds0 = (unsigned)(uintptr_t)lds;
  const char* xsrc = p.x + (size_t)ti * 64 * 1280;
  const char* wsrc = p.wq + tj * 160;
  unsigned vx, vw0, vw1;
  {
    const int t = 8 * wave + (lane >> 3);
    vx = t * 1280 + (((lane & 7) ^ ((t >> 1) & 7)) << 4);
    int e = 64 * wave + lane, rho = e / 10, cc = e - rho * 10;
    vw0 = ((rho & ~7) + ((rho & 1) << 2) + ((rho & 7) >> 1)) * 1280 + cc * 16;
    e = 64 * (8 + (wave & 1)) + lane; rho = e / 10; cc = e - rho * 10;
    vw1 = ((rho & ~7) + ((rho & 1) << 2) + ((rho & 7) >> 1)) * 1280 + cc * 16;
  }
  auto q_issue = [&](int s) {
    const unsigned dst = lds0 + L_Q + (s & 3) * Q_STAGE;
    dma16(vx, xsrc + s * 128, dst + wave * 1024);
    dma16(vw0, wsrc + (size_t)s * 64 * 1280, dst + 8192 + wave * 1024);
    if (wave < 2) dma16(vw1, wsrc + (size_t)s * 64 * 1280, dst + 8192 + (8 + wave) * 1024);
  };
  q_issue(0); q_issue(1); q_issue(2); q_issue(3);
  asm volatile("s_waitcnt lgkmcnt(0)\n\ts_barrier" ::: "memory");
  __builtin_amdgcn_s_setprio(2);
  STAMP(0);
  f32x16 qa = {}, qb = {};
  float lg = 0.f;
  {
    const int tok = 32 * tb + r32;
    lptr bx = lds + L_Q + tok * 128;
    const int sw = (hh ^ ((tok >> 1) & 7)) << 4;
    const int g = lane >> 4, q4 = (lane & 15) >> 2, pp = lane & 3;
    lptr bw = lds + L_Q + 8192 + (8 * (g >> 1) + 2 * q4) * 160 + 80 * hp + (g & 1) * 32 + pp * 8;
    lptr bg = lds + L_GW + hh * 16;
    wait_ge(lds, C_GW, 4);
#pragma unroll 1
    for (int s = 0; s < 10; ++s) {
      if (wave < 2) { if (s == 0) VMCNT(9); else if (s < 8) VMCNT(6); else if (s == 8) VMCNT(3); else VMCNT(0); }
      else          { if (s == 0) VMCNT(6); else if (s < 8) VMCNT(4); else if (s == 8) VMCNT(2); else VMCNT(0); }
      arrive(lds, C_Q);
      wait_ge(lds, C_Q, 8 * (s + 1));
      if (s >= 1 && s + 3 < 10) q_issue(s + 3);
      const int so = (s & 3) * Q_STAGE;
#pragma unroll
      for (int ks = 0; ks < 4; ++ks) {
        bf16x8 xf = ld128(bx + so + (sw ^ (ks * 32)));
        bf16x8 wa = ldtr2(bw + so + ks * 2560, 160);
        bf16x8 wb = ldtr2(bw + so + ks * 2560 + 40, 160);
        u32x4 gwv = *(LAS u32x4*)(bg + s * 128 + ks * 32);
        qa = MFMA32(wa, xf, qa);
        qb = MFMA32(wb, xf, qb);
        u32x4 xu = __builtin_bit_cast(u32x4, xf);
#pragma unroll
        for (int e = 0; e < 4; ++e) {
          const unsigned xe = xu[e], ge = gwv[e];
          lg = __builtin_amdgcn_fdot2_f32_bf16(__builtin_bit_cast(bf16x2_t, xe), __builtin_bit_cast(bf16x2_t, ge), lg, false);
        }
      }
    }
    LGKM0();
    arrive(lds, C_QDONE);
  }
  STAMP(1);
  lg = swap_add(lg);
  lg = bflo(cvtpk(lg, 0.f));
  float gate = __builtin_amdgcn_rcpf(1.f + __builtin_amdgcn_exp2f(-1.4426950408889634f * lg));
  gate = bflo(cvtpk(gate, 0.f));
  if (wave < 2) {
    if (hh == 0) *(LAS float*)(lds + L_OMG + (32 * tb + r32) * 4) = bflo(cvtpk(1.f - gate, 0.f));
    LGKM0();
    arrive(lds, C_OMG);
  }
  constexpr float QS = 0.22360679774997896f * 1.4426950408889634f;
  float na = 0.f, nb = 0.f;
#pragma unroll
  for (int r = 0; r < 12; ++r) {
    qa[r] *= QS; qb[r] *= QS;
    if (r >= 8) { qa[r] = hh ? 0.f : qa[r]; qb[r] = hh ? 0.f : qb[r]; }
    na += qa[r] * qa[r]; nb += qb[r] * qb[r];
  }
  na = swap_add(na); nb = swap_add(nb);
  wait_ge(lds, C_KVRDY, 1);
  STAMP(2);
  const unsigned tag = ldctl(lds, C_TAG);
  const float kmax2 = __uint_as_float(ldctl(lds, C_KMAX));
  const float ba = __builtin_sqrtf(na * kmax2) * 1.01f, bb = __builtin_sqrtf(nb * kmax2) * 1.01f;
  const bool fast = __all(ba <= 40.f && bb <= 40.f);
  u32x4 qa0 = {cvtpk(qa[0], qa[1]), cvtpk(qa[2], qa[3]), cvtpk(qa[4], qa[5]), cvtpk(qa[6], qa[7])};
  u32x4 qb0 = {cvtpk(qb[0], qb[1]), cvtpk(qb[2], qb[3]), cvtpk(qb[4], qb[5]), cvtpk(qb[6], qb[7])};
  u32x4 qa1 = {cvtpk(qa[8], qa[9]), cvtpk(qa[10], qa[11]), (fast && !hh) ? cvtpk(-ba, 0.f) : 0u, 0u};
  u32x4 qb1 = {cvtpk(qb[8], qb[9]), cvtpk(qb[10], qb[11]), (fast && !hh) ? cvtpk(-bb, 0.f) : 0u, 0u};
  const bf16x8 QA0 = __builtin_bit_cast(bf16x8, qa0), QA1 = __builtin_bit_cast(bf16x8, qa1);
  const bf16x8 QB0 = __builtin_bit_cast(bf16x8, qb0), QB1 = __builtin_bit_cast(bf16x8, qb1);

  const char* kvsrc = p.ws + WS_KV;
  const unsigned vlin = lane * 16;
  const unsigned k0b = L_KV + kh * 2048 + lane * 16, k1b = L_KV + 9216 + kh * 512 + r32 * 8;
  const unsigned vb = r32 < 20 ? L_KV + 4096 + kh * 2560 + (20 * hh + r32) * 16 : L_ONES;
  const unsigned vm = r32 < 20 ? 1u : 0u;
  auto kv_issue = [&](int c, int slot) {
    if (wave < 5) {
      dma16_dev(vlin, kvsrc + c * KV_CHUNK + wave * 2048, lds0 + L_KV + slot * KV_CHUNK + wave * 2048);
      dma16_dev(vlin, kvsrc + c * KV_CHUNK + wave * 2048 + 1024, lds0 + L_KV + slot * KV_CHUNK + wave * 2048 + 1024);
    }
  };
  f32x16 oa = {}, ob = {};
  float ma = 0.f, mb = 0.f;
  u32x4 k1u = {0u, 0u, 0x00003f80u, 0u};
  const f32x16 zero16 = {};
#define LDK(t, so)                                                                       \
  do {                                                                                   \
    k0 = ld128(lds + k0b + (so) + (t) * 1024);                                           \
    u32x2 t_ = *(LAS u32x2*)(lds + k1b + (so) + (t) * 256);                              \
    k1u[0] = t_[0]; k1u[1] = t_[1];                                                      \
  } while (0)
#define LDV(t, va) do { v0 = ld128(lds + (va) + (t) * 1280); v1 = ld128(lds + (va) + (t) * 1280 + 640); } while (0)
#define K1 __builtin_bit_cast(bf16x8, k1u)
  bf16x8 k0, v0, v1;
  if (fast) {
    f32x16 sa, sb;
    u32x4 pa0, pa1, pb0 = {}, pb1 = {};
    v0 = bf16x8{}; v1 = bf16x8{};
    LDK(0, 0);
    sa = MFMA32(k0, QA0, zero16); sa = MFMA32(K1, QA1, sa);
#define EX(v) __builtin_amdgcn_exp2f(v)
#define E4(S, i) S[i] = EX(S[i]); S[i + 1] = EX(S[i + 1]); S[i + 2] = EX(S[i + 2]); S[i + 3] = EX(S[i + 3]);
#define C2(P, j, S, i) P[j] = cvtpk(S[i], S[i + 1]); P[j + 1] = cvtpk(S[i + 2], S[i + 3]);
#define BF(P) __builtin_bit_cast(bf16x8, P)
#define HALF(S, P0, P1, N, QN0, QN1, O, PO0, PO1, MID, END)                              \
  do {                                                                                   \
    N = MFMA32(k0, QN0, zero16); E4(S, 0) PIN(S); SBAR();                                \
    N = MFMA32(K1, QN1, N); E4(S, 4) C2(P0, 0, S, 0) PIN(S); PIN(P0); SBAR();            \
    MID;                                                                                 \
    O = MFMA32(v0, BF(PO0), O); E4(S, 8) C2(P0, 2, S, 4) PIN(S); PIN(P0); SBAR();        \
    O = MFMA32(v1, BF(PO1), O); E4(S, 12) C2(P1, 0, S, 8) PIN(S); PIN(P1); SBAR();       \
    END;                                                                                 \
    C2(P1, 2, S, 12) PIN(P1); SBAR();                                                    \
  } while (0)
    int so = 0;
#pragma unroll 1
    for (int c = 0; c < 16; ++c) {
      const int sn = so == 2 * KV_CHUNK ? 0 : so + KV_CHUNK;
      const int sp = sn == 2 * KV_CHUNK ? 0 : sn + KV_CHUNK;
      const unsigned va = vb + vm * so;
      HALF(sa, pa0, pa1, sb, QB0, QB1, ob, pb0, pb1, { LDK(1, so); SBAR(); }, { LDV(0, va); SBAR(); });
      if (c > 0) arrive(lds, C_FREE);
      VMCNT(0);
      arrive(lds, C_FULL);
      HALF(sb, pb0, pb1, sa, QA0, QA1, oa, pa0, pa1, {}, {});
      wait_ge(lds, C_FULL, 8 * (c + 1));
      if (c + 2 < 16) { wait_ge(lds, C_FREE, 8 * c); kv_issue(c + 2, sp / KV_CHUNK); }
      HALF(sa, pa0, pa1, sb, QB0, QB1, ob, pb0, pb1, { LDK(0, sn); SBAR(); }, { LDV(1, va); SBAR(); });
      HALF(sb, pb0, pb1, sa, QA0, QA1, oa, pa0, pa1, {}, {});
      so = sn;
    }
    ob = MFMA32(v0, BF(pb0), ob); ob = MFMA32(v1, BF(pb1), ob);
  } else {
    ma = -INFINITY; mb = -INFINITY;
    int so = 0;
#pragma unroll 1
    for (int c = 0; c < 16; ++c) {
      const int sn = so == 2 * KV_CHUNK ? 0 : so + KV_CHUNK;
      const int sp = sn == 2 * KV_CHUNK ? 0 : sn + KV_CHUNK;
      const unsigned va = vb + vm * so;
#pragma unroll 1
      for (int t = 0; t < 2; ++t) {
        LDK(t, so); LDV(t, va);
#pragma unroll
        for (int w = 0; w < 2; ++w) {
          f32x16 s = MFMA32(k0, w ? QB0 : QA0, zero16); s = MFMA32(K1, w ? QB1 : QA1, s);
          float m = s[0];
#pragma unroll
          for (int r = 1; r < 16; ++r) m = __builtin_fmaxf(m, s[r]);
          const float mo = w ? mb : ma;
          m = __builtin_fmaxf(swap_max(m), mo);
          const float al = EX(mo - m);
          u32x4 p0, p1;
#pragma unroll
          for (int r = 0; r < 4; ++r) { p0[r] = cvtpk(EX(s[2 * r] - m), EX(s[2 * r + 1] - m)); p1[r] = cvtpk(EX(s[8 + 2 * r] - m), EX(s[9 + 2 * r] - m)); }
          if (w) { ob *= al; ob = MFMA32(v0, BF(p0), ob); ob = MFMA32(v1, BF(p1), ob); mb = m; }
          else   { oa *= al; oa = MFMA32(v0, BF(p0), oa); oa = MFMA32(v1, BF(p1), oa); ma = m; }
        }
        if (t == 0) {
          if (c > 0) arrive(lds, C_FREE);
          VMCNT(0);
          arrive(lds, C_FULL);
          if (c + 2 < 16) { wait_ge(lds, C_FREE, 8 * c); kv_issue(c + 2, sp / KV_CHUNK); }
        }
      }
      wait_ge(lds, C_FULL, 8 * (c + 1));
      so = sn;
    }
  }
  STAMP(3);
  LGKM0();
  arrive(lds, C_FREE);
  wait_ge(lds, C_FREE, 8 * 16);
  {
    const f32x16 snd = kh ? oa : ob;
    lptr xw = lds + L_KV + wave * 3328;
    *(LAS f32x4*)(xw + lane * 16) = (f32x4){snd[0], snd[1], snd[2], snd[3]};
    *(LAS f32x4*)(xw + 1024 + lane * 16) = (f32x4){snd[4], snd[5], snd[6], snd[7]};
    *(LAS f32x4*)(xw + 2048 + lane * 16) = (f32x4){snd[8], snd[9], snd[10], snd[11]};
    *(LAS float*)(xw + 3072 + lane * 4) = kh ? ma : mb;
    LGKM0();
    arrive(lds, C_X);
    wait_ge(lds, C_X, 8);
    lptr xr = lds + L_KV + (wave ^ 4) * 3328;
    const f32x4 r0 = *(LAS f32x4*)(xr + lane * 16), r1 = *(LAS f32x4*)(xr + 1024 + lane * 16), r2 = *(LAS f32x4*)(xr + 2048 + lane * 16);
    const float mr = *(LAS float*)(xr + 3072 + lane * 4);
    const f32x16 own = kh ? ob : oa;
    const float mo = kh ? mb : ma, mm = __builtin_fmaxf(mo, mr);
    const float fo = EX(mo - mm), fr = EX(mr - mm);
    float o[12];
#pragma unroll
    for (int r = 0; r < 4; ++r) { o[r] = own[r] * fo + r0[r] * fr; o[4 + r] = own[4 + r] * fo + r1[r] * fr; o[8 + r] = own[8 + r] * fo + r2[r] * fr; }
    const float sc = gate / upper_half(o[8]);
    const int P = 2 * tj + hp;
    const int t16 = 4 * ti + 2 * tb + (r32 >> 4);
    char* ob_ = p.ws + WS_O;
    const unsigned base = (t16 * 80 + 20 * (P >> 2) + (P & 3)) * 256 + (r32 & 15) * 16;
    st16_dev(ob_, base + (kh + 3 * hh) * 1024,
             (u32x4){cvtpk(o[0] * sc, o[1] * sc), cvtpk(o[2] * sc, o[3] * sc), cvtpk(o[4] * sc, o[5] * sc), cvtpk(o[6] * sc, o[7] * sc)});
    if (hh == 0) st8_dev(ob_, base + 2048 + kh * 8, (u32x2){cvtpk(o[8] * sc, o[9] * sc), cvtpk(o[10] * sc, o[11] * sc)});
    VMCNT(0);
    if (lane == 0) st4_dev(p.ws + WS_OF, ((ti * 8 + tj) * 8 + wave) * 4, tag);
  }
  STAMP(4);
}

__device__ __forceinline__ void role_g(const Params& p, lptr lds, const int wave, const int lane, const int ti, const int tj,
                                       const int fI, const int fc) {
  const int gw = wave - 8;
  const unsigned lds0 = (unsigned)(uintptr_t)lds;
  const int r32 = lane & 31, hh = lane >> 5;
  const int g4 = lane >> 4, i16 = lane & 15, q4 = i16 >> 2, pp = lane & 3;
  const int t16 = 4 * ti + (tj >> 1), kind = tj & 1;
  unsigned tag = 0;
  {
    u32x4 gv = {};
    const int gi = gw * 64 + lane;
    if (gi < 80) gv = *(const GAS u32x4*)(p.gw + gi * 16);
    if (gw == 0) tag = __hip_atomic_load((GAS unsigned*)(p.ws + WS_EPOCH), __ATOMIC_RELAXED, __HIP_MEMORY_SCOPE_AGENT) + 1u;
    bf16x8 xf[5];
    const char* xs = p.x + (size_t)(16 * t16 + i16) * 1280 + gw * 320 + g4 * 16;
#pragma unroll
    for (int st = 0; st < 5; ++st) xf[st] = *(const GAS bf16x8*)(xs + st * 64);
    const char* ws_ = (kind ? p.wv : p.wk) + gw * 6400;
    const unsigned dst = lds0 + L_G + gw * 7168;
#pragma unroll
    for (int q = 0; q < 6; ++q) dma16(lane * 16, ws_ + q * 1024, dst + q * 1024);
    if (lane < 16) dma16(lane * 16, ws_ + 6144, dst + 6144);
    if (gw == 3) {
      if (lane < 32) *(LAS unsigned*)(lds + L_CTRL + lane * 4) = 0u;
      if (lane < 16) *(LAS unsigned*)(lds + L_ONES + (lane >> 2) * 640 + (lane & 3) * 4) = 0x3f803f80u;
    }
    asm volatile("s_waitcnt lgkmcnt(0)\n\ts_barrier" ::: "memory");
    STAMP(0);
    if (gi < 80) *(LAS u32x4*)(lds + L_GW + gi * 16) = gv;
    if (gw == 0 && lane == 0) *(LAS unsigned*)(lds + L_CTRL + 4 * C_TAG) = tag;
    LGKM0();
    arrive(lds, C_GW);
    VMCNT(0);
    f32x4 d0 = {}, d1 = {};
    lptr bw = lds + L_G + gw * 7168 + (8 * g4 + q4) * 40;
#pragma unroll
    for (int st = 0; st < 5; ++st) {
      bf16x8 w0 = ldtr2(bw + st * 1280 + pp * 8, 160), w1 = ldtr2(bw + st * 1280 + 32, 160);
      if (kind) { d0 = MFMA16(xf[st], w0, d0); d1 = MFMA16(xf[st], w1, d1); }
      else      { d0 = MFMA16(w0, xf[st], d0); d1 = MFMA16(w1, xf[st], d1); }
    }
    lptr sc = lds + L_G + 28672;
    *(LAS f32x4*)(sc + gw * 2048 + lane * 16) = d0;
    *(LAS f32x4*)(sc + gw * 2048 + 1024 + lane * 16) = d1;
    LGKM0();
    arrive(lds, C_G);
    if (gw == 0) {
      wait_ge(lds, C_G, 4);
#pragma unroll
      for (int w = 1; w < 4; ++w) { d0 += *(LAS f32x4*)(sc + w * 2048 + lane * 16); d1 += *(LAS f32x4*)(sc + w * 2048 + 1024 + lane * 16); }
      const u32x2 e0 = {cvtpk(d0[0], d0[1]), cvtpk(d0[2], d0[3])}, e1 = {cvtpk(d1[0], d1[1]), cvtpk(d1[2], d1[3])};
      const int key0 = 16 * t16, u = 2 * (key0 >> 10) + ((key0 >> 5) & 1), s16 = (key0 >> 4) & 1;
      char* cb = p.ws + WS_KV + ((key0 & 1023) >> 6) * KV_CHUNK;
      float n2 = 0.f;
      if (kind) {
        const unsigned o = 4096 + (2 * u + s16) * 640 + (g4 & 1) * 320 + (g4 >> 1) * 8;
        st8_dev(cb, o + i16 * 16, e0);
        if (i16 < 4) st8_dev(cb, o + (16 + i16) * 16, e1);
      } else {
        const int key = 16 * s16 + i16;
        st8_dev(cb, u * 1024 + ((g4 & 1) * 32 + key) * 16 + (g4 >> 1) * 8, e0);
        if (g4 == 0) st8_dev(cb, 9216 + u * 256 + key * 8, e1);
        n2 = bflo(e0[0]) * bflo(e0[0]) + bfhi(e0[0]) * bfhi(e0[0]) + bflo(e0[1]) * bflo(e0[1]) + bfhi(e0[1]) * bfhi(e0[1]);
        if (g4 == 0) n2 += bflo(e1[0]) * bflo(e1[0]) + bfhi(e1[0]) * bfhi(e1[0]) + bflo(e1[1]) * bflo(e1[1]) + bfhi(e1[1]) * bfhi(e1[1]);
        n2 += __shfl_xor(n2, 16); n2 += __shfl_xor(n2, 32);
#pragma unroll
        for (int m = 1; m < 16; m <<= 1) { const float o2 = __shfl_xor(n2, m); n2 = (o2 > n2 || o2 != o2) ? o2 : n2; }
      }
      LGKM0();
      arrive(lds, C_G);
      VMCNT(0);
      if (lane == 0) st8_dev(p.ws + WS_KVF, (ti * 8 + tj) * 8, (u32x2){tag, __float_as_uint(n2)});
    } else {
      arrive(lds, C_G);
    }
    STAMP(1);
    wait_ge(lds, C_GW, 4);
    tag = ldctl(lds, C_TAG);
    if (gw == 1) {
      float km = 0.f;
#pragma unroll 1
      for (unsigned s = 0; s < SPIN_GLB; ++s) {
        u32x4 f0, f1;
        asm volatile("global_load_dwordx4 %0, %2, %3 sc1\n\tglobal_load_dwordx4 %1, %2, %3 offset:16 sc1\n\ts_waitcnt vmcnt(0)"
                     : "=&v"(f0), "=&v"(f1) : "v"(lane * 32), "s"(p.ws + WS_KVF) : "memory");
        const bool ok = f0[0] == tag && f0[2] == tag && f1[0] == tag && f1[2] == tag;
        if (__all(ok)) {
          const float a = __uint_as_float(f0[1]), b = __uint_as_float(f0[3]), c = __uint_as_float(f1[1]), d = __uint_as_float(f1[3]);
          km = __builtin_fmaxf(__builtin_fmaxf(a, b), __builtin_fmaxf(c, d));
          if (a != a || b != b || c != c || d != d) km = INFINITY;
          break;
        }
        __builtin_amdgcn_s_sleep(4);
      }
      STAMP(2);
#pragma unroll
      for (int q = 0; q < 20; ++q) dma16_dev(lane * 16, p.ws + WS_KV + q * 1024, lds0 + L_KV + q * 1024);
#pragma unroll
      for (int m = 1; m < 64; m <<= 1) km = __builtin_fmaxf(km, __shfl_xor(km, m));
      if (lane == 0) *(LAS float*)(lds + L_CTRL + 4 * C_KMAX) = km;
      if (blockIdx.x == 0 && lane == 0) st4_dev(p.ws + WS_EPOCH, 0, tag);
      VMCNT(0);
      LGKM0();
      arrive(lds, C_KVRDY);
    }
  }
  unsigned gs = 8;
  const int tm = gw & 1, fh = gw >> 1;
  f32x16 acc[2][3];
#pragma unroll
  for (int a = 0; a < 2; ++a)
#pragma unroll
    for (int f = 0; f < 3; ++f) acc[a][f] = f32x16{};
  {
    const char* xsrc = p.x + (size_t)(fI * 128 + gw * 32) * 1280;
    const char* gsrc = p.wg + (size_t)gw * 16 * 3072 + fc * 192;
    const char* usrc = p.wu + (size_t)gw * 16 * 3072 + fc * 192;
    unsigned vx[2], vw[3];
#pragma unroll
    for (int m = 0; m < 2; ++m) { const int t = 8 * m + (lane >> 3); vx[m] = t * 1280 + (((lane & 7) ^ ((t >> 1) & 7)) << 4); }
#pragma unroll
    for (int q = 0; q < 3; ++q) { const int e = 64 * q + lane, rr = e / 12, cc = e - rr * 12; vw[q] = rr * 3072 + cc * 16; }
    auto up_issue = [&](int s, int slot) {
      const unsigned dst = lds0 + L_G + slot * UP_STAGE;
#pragma unroll
      for (int m = 0; m < 4; ++m) dma16(vx[m & 1], xsrc + s * 128 + (m >> 1) * 16 * 1280, dst + gw * 4096 + m * 1024);
#pragma unroll
      for (int q = 0; q < 3; ++q) dma16(vw[q], gsrc + (size_t)s * 64 * 3072, dst + 16384 + gw * 3072 + q * 1024);
#pragma unroll
      for (int q = 0; q < 3; ++q) dma16(vw[q], usrc + (size_t)s * 64 * 3072, dst + 28704 + gw * 3072 + q * 1024);
    };
    wait_ge(lds, C_G, gs);
    up_issue(0, 0);
    wait_ge(lds, C_QDONE, 8);
    STAMP(3);
    up_issue(1, 1);
    const int tok = 64 * tm + r32;
    lptr bx = lds + L_G + tok * 128;
    const int sw = (hh ^ ((tok >> 1) & 7)) << 4;
    lptr bw = lds + L_G + 16384 + (g4 & 1) * 12320 + (8 * (g4 >> 1) + q4) * 192 + fh * 96 + pp * 8;
    int slot = 0;
#pragma unroll 1
    for (int s = 0; s < 10; ++s) {
      if (s < 9) VMCNT(10); else VMCNT(0);
      arrive(lds, C_G); gs += 4;
      wait_ge(lds, C_G, gs);
      if (s + 2 < 10) up_issue(s + 2, slot == 0 ? 2 : slot - 1);
      const int so = slot * UP_STAGE;
#pragma unroll
      for (int ks = 0; ks < 4; ++ks) {
        bf16x8 x0 = ld128(bx + so + (sw ^ (ks * 32))), x1 = ld128(bx + so + 4096 + (sw ^ (ks * 32)));
#pragma unroll
        for (int f = 0; f < 3; ++f) {
          bf16x8 wf = ldtr2(bw + so + ks * 3072 + f * 32, 768);
          acc[0][f] = MFMA32(wf, x0, acc[0][f]);
          acc[1][f] = MFMA32(wf, x1, acc[1][f]);
        }
      }
      slot = slot == 2 ? 0 : slot + 1;
    }
    LGKM0();
    arrive(lds, C_G); gs += 4;
    STAMP(4);
    constexpr float C0 = -2.f * 0.7978845608028654f * 1.4426950408889634f, C1 = C0 * 0.044715f;
    char* hb = p.ws + WS_H;
#pragma unroll
    for (int a = 0; a < 2; ++a)
#pragma unroll
      for (int f = 0; f < 3; ++f) {
        float hv[8];
#pragma unroll
        for (int r = 0; r < 8; ++r) {
          const float g = acc[a][f][r], u = acc[a][f][r + 8];
          const float e = __builtin_amdgcn_exp2f(g * __builtin_fmaf(g * g, C1, C0));
          hv[r] = g * u * __builtin_amdgcn_rcpf(1.f + e);
        }
        const unsigned off = ((fI * 4 + tm * 2 + a) * 192 + fc * 12 + (3 * fh + f) * 2) * 512 + lane * 16;
        st16_dev(hb, off, (u32x4){cvtpk(hv[0], hv[1]), cvtpk(hv[2], hv[3]), cvtpk(hv[4], hv[5]), cvtpk(hv[6], hv[7])});
      }
    VMCNT(0);
    if (lane == 0) st4_dev(p.ws + WS_HF, (((fI * 2 + tm) * 16 + fc) * 2 + fh) * 4, tag);
    STAMP(5);
  }
#pragma unroll
  for (int a = 0; a < 2; ++a)
#pragma unroll
    for (int f = 0; f < 3; ++f) acc[a][f] = f32x16{};
  unsigned vw5[5];
  {
#pragma unroll
    for (int q = 0; q < 5; ++q) {
      const int e = 64 * q + lane, rho = e / 10, cc = e - rho * 10;
      vw5[q] = (16 * (rho >> 4) + 8 * (rho & 1) + 4 * ((rho >> 3) & 1) + ((rho >> 1) & 3)) * 1280 + cc * 16;
    }
    const char* wsrc = p.wd + (size_t)gw * 384 * 1280 + tj * 160;
    const char* hsrc = p.ws + WS_H + (size_t)(2 * ti * 192 + gw * 48) * 512;
    const unsigned ring = lds0 + L_G + gw * DN_WAVE;
    auto dn_issue = [&](int st, int slot) {
      const unsigned dst = ring + slot * DN_SLOT;
#pragma unroll
      for (int q = 0; q < 5; ++q) dma16(vw5[q], wsrc + (size_t)st * 32 * 1280, dst + q * 1024);
#pragma unroll
      for (int m = 0; m < 4; ++m) dma16_dev(lane * 16, hsrc + st * 2048 + (m >> 1) * 192 * 512 + (m & 1) * 1024, dst + 5120 + m * 1024);
    };
#pragma unroll 1
    for (unsigned s = 0; s < SPIN_GLB; ++s) {
      unsigned f;
      asm volatile("global_load_dword %0, %1, %2 sc1\n\ts_waitcnt vmcnt(0)" : "=&v"(f) : "v"((ti * 32 + gw * 8 + (lane & 7)) * 4), "s"(p.ws + WS_HF) : "memory");
      if (__all(f == tag)) break;
      __builtin_amdgcn_s_sleep(8);
    }
    wait_ge(lds, C_G, gs);
    STAMP(6);
    dn_issue(0, 0); dn_issue(1, 1);
    lptr bw = lds + L_G + gw * DN_WAVE + (8 * (g4 >> 1) + 2 * q4) * 160 + (g4 & 1) * 32 + pp * 8;
    lptr bh = lds + L_G + gw * DN_WAVE + 5120 + lane * 16;
    int slot = 0;
#pragma unroll 1
    for (int st = 0; st < 12; ++st) {
      if (st < 11) VMCNT(9); else VMCNT(0);
      if (st + 2 < 12) dn_issue(st + 2, slot == 0 ? 2 : slot - 1);
      const int so = slot * DN_SLOT;
#pragma unroll
      for (int ks = 0; ks < 2; ++ks) {
        bf16x8 h0 = ld128(bh + so + ks * 1024), h1 = ld128(bh + so + 2048 + ks * 1024);
#pragma unroll
        for (int f = 0; f < 3; ++f) {
          bf16x8 wf = ldtr2(bw + so + ks * 2560 + f * 64, 160);
          acc[0][f] = MFMA32(wf, h0, acc[0][f]);
          acc[1][f] = MFMA32(wf, h1, acc[1][f]);
        }
      }
      LGKM0();
      slot = slot == 2 ? 0 : slot + 1;
    }
  }
  STAMP(7);
  f32x4 res[5];
  {
    arrive(lds, C_G); gs += 4;
    wait_ge(lds, C_G, gs);
    lptr sw_ = lds + L_G + gw * (64 * 336) + r32 * 336 + hh * 16;
#pragma unroll
    for (int a = 0; a < 2; ++a)
#pragma unroll
      for (int f = 0; f < 3; ++f)
#pragma unroll
        for (int rq = 0; rq < (f < 2 ? 4 : 2); ++rq)
          *(LAS f32x4*)(sw_ + a * 32 * 336 + f * 128 + rq * 32) =
              (f32x4){acc[a][f][4 * rq], acc[a][f][4 * rq + 1], acc[a][f][4 * rq + 2], acc[a][f][4 * rq + 3]};
    LGKM0();
    arrive(lds, C_G); gs += 4;
    wait_ge(lds, C_G, gs);
    lptr sr = lds + L_G + (16 * gw + i16) * 336 + g4 * 16;
#pragma unroll
    for (int b = 0; b < 5; ++b) {
      res[b] = *(LAS f32x4*)(sr + b * 64);
#pragma unroll
      for (int w = 1; w < 4; ++w) res[b] += *(LAS f32x4*)(sr + w * (64 * 336) + b * 64);
    }
    wait_ge(lds, C_OMG, 2);
    const float omg = *(LAS float*)(lds + L_OMG + (16 * gw + i16) * 4);
#pragma unroll
    for (int b = 0; b < 5; ++b) res[b] *= omg;
    LGKM0();
    arrive(lds, C_G); gs += 4;
    wait_ge(lds, C_G, gs);
  }
  {
    const char* wsrc = p.wo + (size_t)gw * 160 * 1280 + tj * 160;
    unsigned vb_[5]; bool vu[5];
#pragma unroll
    for (int q = 0; q < 5; ++q) {
      const int e = 64 * q + lane, rho = e / 10, cc = e - rho * 10;
      const int g = 2 * (rho >> 4) + ((rho >> 2) & 1);
      vb_[q] = (40 * g + (rho & 3)) * 1280 + cc * 16;
      vu[q] = (rho >> 3) & 1;
    }
    constexpr int QD[5][2] = {{0, 2}, {5, 7}, {4, 9}, {1, 3}, {6, 8}};
#pragma unroll
    for (int m = 0; m < 5; ++m)
#pragma unroll
      for (int q = 0; q < 5; ++q)
        dma16(vb_[q] + (vu[q] ? QD[m][1] : QD[m][0]) * 5120, wsrc, lds0 + L_G + gw * 25600 + m * 5120 + q * 1024);
    VMCNT(0);
    arrive(lds, C_G); gs += 4;
    STAMP(8);
#pragma unroll 1
    for (unsigned s = 0; s < SPIN_GLB; ++s) {
      unsigned f;
      asm volatile("global_load_dword %0, %1, %2 sc1\n\ts_waitcnt vmcnt(0)" : "=&v"(f) : "v"((ti * 64 + lane) * 4), "s"(p.ws + WS_OF) : "memory");
      if (__all(f == tag)) break;
      __builtin_amdgcn_s_sleep(2);
    }
    STAMP(9);
    const __amdgpu_buffer_rsrc_t orsrc = __builtin_amdgcn_make_buffer_rsrc(p.ws + WS_O, 0, SEQ * DM * 2, 0x00020000);
    u32x4 of[20];
#pragma unroll
    for (int st = 0; st < 20; ++st) of[st] = __builtin_amdgcn_raw_buffer_load_b128(orsrc, lane * 16, ((4 * ti + gw) * 80 + 4 * st) * 256, 16);
    wait_ge(lds, C_G, gs);
    lptr bw = lds + L_G + (16 * (g4 >> 1) + 4 * (g4 & 1) + q4) * 160 + pp * 8;
#pragma unroll
    for (int st = 0; st < 20; ++st)
#pragma unroll
      for (int b = 0; b < 5; ++b) res[b] = MFMA16(ldtr2(bw + st * 5120 + b * 32, 1280), __builtin_bit_cast(bf16x8, of[st]), res[b]);
    char* op = p.out + (size_t)(64 * ti + 16 * gw + i16) * 1280 + tj * 160 + g4 * 8;
#pragma unroll
    for (int b = 0; b < 5; ++b) *(GAS u32x2*)(op + b * 32) = (u32x2){cvtpk(res[b][0], res[b][1]), cvtpk(res[b][2], res[b][3])};
  }
  STAMP(10);
}

__global__ __launch_bounds__(768) void gated_mix_fused(Params p) {
  extern __shared__ __attribute__((aligned(16))) char smem[];
  lptr lds = (lptr)smem;
  const int wave = __builtin_amdgcn_readfirstlane(threadIdx.x >> 6), lane = threadIdx.x & 63;
  const int xcd = blockIdx.x & 7, r = blockIdx.x >> 3;
  const int ti = (xcd >> 1) * 8 + (r >> 2), tj = (xcd & 1) * 4 + (r & 3);
  const int fI = (xcd >> 1) * 4 + (r >> 3), fc = (xcd & 1) * 8 + (r & 7);
  if (wave < 8) role_a(p, lds, wave, lane, ti, tj);
  else role_g(p, lds, wave, lane, ti, tj, fI, fc);
}

extern "C" void kernel_launch(void* const* d_in, const int* in_sizes, int n_in, void* d_out, int out_size, void* d_ws,
                              size_t ws_size, hipStream_t stream) {
  Params p;
  p.x = (const char*)d_in[0]; p.wq = (const char*)d_in[1]; p.wk = (const char*)d_in[2]; p.wv = (const char*)d_in[3];
  p.wo = (const char*)d_in[4]; p.wg = (const char*)d_in[6]; p.wu = (const char*)d_in[7]; p.wd = (const char*)d_in[8];
  p.gw = (const char*)d_in[9]; p.out = (char*)d_out; p.ws = (char*)d_ws;
  (void)hipFuncSetAttribute((const void*)gated_mix_fused, hipFuncAttributeMaxDynamicSharedMemorySize, LDS_BYTES);
  hipLaunchKernelGGL(gated_mix_fused, dim3(256), dim3(768), LDS_BYTES, stream, p);
}
```

```cpp
#include <hip/hip_runtime.h>
#include <hip/hip_bf16.h>
#include <stdint.h>

typedef __attribute__((ext_vector_type(8))) short bf16x8;
typedef __attribute__((ext_vector_type(4))) short s16x4;
typedef __attribute__((ext_vector_type(2))) __bf16 bf16x2_t;
typedef __attribute__((ext_vector_type(16))) float f32x16;
typedef __attribute__((ext_vector_type(4))) float f32x4;
typedef __attribute__((ext_vector_type(4))) unsigned u32x4;
typedef __attribute__((ext_vector_type(2))) unsigned u32x2;
#define LAS __attribute__((address_space(3)))
#define GAS __attribute__((address_space(1)))
typedef LAS char* lptr;

constexpr size_t WS_EPOCH = 0, WS_KVF = 4096, WS_TF = 65536, WS_KV = 131072, WS_HF = 320u << 10, WS_OF = 512u << 10,
                 WS_H = 1u << 20, WS_O = 8u << 20, WS_WT = 12u << 20;
constexpr int L_G = 0;
constexpr int UPX0 = 0, UPX1 = 45056;
constexpr int UPW0 = 16384, UPW1 = 61440, UPW2 = 86048;
constexpr int L_Q = 45056;
constexpr int Q_STAGE = 18432;
constexpr int L_MISC = 118784;
constexpr int L_CTRL = L_MISC;
constexpr int L_GATE = L_MISC + 112;
constexpr int L_GW = L_MISC + 624;
constexpr int L_ONES = L_MISC + 1904;
constexpr int L_DUMMY = L_GW;
constexpr int L_KV = 122880;
constexpr int KV_CHUNK = 10240;
constexpr int L_QI = L_KV + 3 * KV_CHUNK;
constexpr int LDS_BYTES = L_KV + 4 * KV_CHUNK;
static_assert(L_ONES + 3 * 640 + 32 <= L_KV && UPW2 + 24608 <= L_MISC && L_Q + 4 * Q_STAGE <= L_MISC, "lds map");
constexpr int DN_WAVE = 27648;
enum { C_GW = 0, C_Q, C_QDONE, C_QI, C_KVRDY, C_KV, C_G, C_X, C_SEEN, C_PAD, C_TAG, C_KMAX, C_P };
static_assert(C_TAG % 2 == 0, "tag / kmax pair");

#define MFMA32(a, b, c) __builtin_amdgcn_mfma_f32_32x32x16_bf16(a, b, c, 0, 0, 0)
#define MFMA16(a, b, c) __builtin_amdgcn_mfma_f32_16x16x32_bf16(a, b, c, 0, 0, 0)
#define SBAR() __builtin_amdgcn_sched_barrier(0)
#define PIN(x) asm volatile("" : "+v"(x))
#define VMCNT(n) asm volatile("s_waitcnt vmcnt(" #n ")" ::: "memory")

struct Params {
  const char *x, *wq, *wk, *wv, *wo, *wg, *wu, *wd, *gw;
  char* out;
  char* ws;
};

__device__ __forceinline__ unsigned cvtpk(float lo, float hi) {
  typedef __attribute__((ext_vector_type(2))) float f32x2;
  bf16x2_t r = __builtin_convertvector((f32x2){lo, hi}, bf16x2_t);
  return __builtin_bit_cast(unsigned, r);
}
__device__ __forceinline__ float bflo(unsigned u) { return __builtin_bit_cast(float, u << 16); }
__device__ __forceinline__ float bfhi(unsigned u) { return __builtin_bit_cast(float, u & 0xffff0000u); }
__device__ __forceinline__ void dma16(unsigned voff, const char* base, unsigned lds) {
  asm volatile("s_mov_b32 m0, %2\n\ts_nop 0\n\tglobal_load_lds_dwordx4 %0, %1" ::"v"(voff), "s"(base), "s"(lds) : "memory");
}
__device__ __forceinline__ void dma16_dev(unsigned voff, const char* base, unsigned lds) {
  asm volatile("s_mov_b32 m0, %2\n\ts_nop 0\n\tglobal_load_lds_dwordx4 %0, %1 sc1" ::"v"(voff), "s"(base), "s"(lds) : "memory");
}
__device__ __forceinline__ void dma16_half(unsigned voff, const char* base, unsigned lds) {
  asm volatile("s_mov_b32 m0, %2\n\ts_mov_b32 exec_hi, 0\n\tglobal_load_lds_dwordx4 %0, %1\n\ts_mov_b32 exec_hi, -1" ::"v"(voff), "s"(base), "s"(lds) : "memory");
}
template <int OFF> __device__ __forceinline__ void dma16n(unsigned voff, const char* base) {
  asm volatile("global_load_lds_dwordx4 %0, %1 offset:%2" ::"v"(voff), "s"(base), "i"(OFF) : "memory");
}
template <int OFF> __device__ __forceinline__ void dma16n_dev(unsigned voff, const char* base) {
  asm volatile("global_load_lds_dwordx4 %0, %1 offset:%2 sc1" ::"v"(voff), "s"(base), "i"(OFF) : "memory");
}
template <int N> __device__ __forceinline__ void vmcnt() { asm volatile("s_waitcnt vmcnt(%0)" ::"i"(N) : "memory"); }
__device__ __forceinline__ void st16_dev(char* base, unsigned voff, u32x4 v) {
  asm volatile("global_store_dwordx4 %0, %1, %2 sc1\n\ts_nop 1" ::"v"(voff), "v"(v), "s"(base) : "memory");
}
__device__ __forceinline__ void st8_dev(char* base, unsigned voff, u32x2 v) {
  asm volatile("global_store_dwordx2 %0, %1, %2 sc1" ::"v"(voff), "v"(v), "s"(base) : "memory");
}
__device__ __forceinline__ void st4_dev(char* base, unsigned voff, unsigned v) {
  asm volatile("global_store_dword %0, %1, %2 sc1" ::"v"(voff), "v"(v), "s"(base) : "memory");
}
__device__ __forceinline__ bf16x8 ld128(lptr p) { return *(LAS bf16x8*)p; }
__device__ __forceinline__ s16x4 ldtr(lptr p) {
  typedef short v4i16_t __attribute__((ext_vector_type(4)));
  return __builtin_amdgcn_ds_read_tr16_b64_v4i16((LAS v4i16_t*)p);
}
__device__ __forceinline__ bf16x8 ldtr2(lptr p, int d) {
  s16x4 a = ldtr(p), b = ldtr(p + d);
  return __builtin_shufflevector(a, b, 0, 1, 2, 3, 4, 5, 6, 7);
}
struct WT { unsigned a, b; };
__device__ __forceinline__ void arrive_(lptr lds, int c, WT& t) {
  asm volatile("v_mov_b32 %0, %2\n\tv_mov_b32 %1, 1\n\ts_mov_b64 exec, 1\n\tds_add_u32 %0, %1\n\ts_mov_b64 exec, -1"
               : "+v"(t.a), "+v"(t.b) : "s"((unsigned)(uintptr_t)(lds + L_CTRL + 4 * c)) : "memory");
}
#define arrive(l, c) arrive_(l, c, wtmp)
constexpr unsigned SPIN_LDS = 1u << 20, SPIN_GLB = 1u << 17;
__device__ __forceinline__ void wait_ge_(lptr lds, int c, unsigned target, WT& t) {
  unsigned sv, cnt;
  asm volatile(
      "v_mov_b32 %1, %4\n\t"
      "s_mov_b32 %3, %6\n"
      ".Lw%=:\n\t"
      "ds_read_b32 %0, %1\n\t"
      "s_waitcnt lgkmcnt(0)\n\t"
      "v_readfirstlane_b32 %2, %0\n\t"
      "s_cmp_ge_u32 %2, %5\n\t"
      "s_cbranch_scc1 .Lx%=\n\t"
      "s_sleep 1\n\t"
      "s_sub_u32 %3, %3, 1\n\t"
      "s_cbranch_scc0 .Lw%=\n"
      ".Lx%=:"
      : "+v"(t.a), "+v"(t.b), "=&s"(sv), "=&s"(cnt)
      : "s"((unsigned)(uintptr_t)(lds + L_CTRL + 4 * c)), "s"(target), "i"(SPIN_LDS)
      : "memory", "scc");
}
#define wait_ge(l, c, t) wait_ge_(l, c, t, wtmp)
__device__ __forceinline__ float swap_add(float v) {
  auto r = __builtin_amdgcn_permlane32_swap(__float_as_uint(v), __float_as_uint(v), false, false);
  return __uint_as_float(r[0]) + __uint_as_float(r[1]);
}
__device__ __forceinline__ float swap_max(float v) {
  auto r = __builtin_amdgcn_permlane32_swap(__float_as_uint(v), __float_as_uint(v), false, false);
  return __builtin_fmaxf(__uint_as_float(r[0]), __uint_as_float(r[1]));
}
__device__ __forceinline__ float upper_half(float v) {
  auto r = __builtin_amdgcn_permlane32_swap(__float_as_uint(v), __float_as_uint(v), false, false);
  return __uint_as_float(r[1]);
}

#define ATTN_LOOP \
  "ds_read_b128 v[76:79], v140 offset:0\n\tds_read_b64 v[80:81], v141 offset:0\n\ts_cmp_ge_u32 %[dma], 5\n\ts_cbranch_scc1 .Lp%=\n\t" \
  "s_add_u32 m0, %[dst], 20480\n\ts_nop 0\n\tglobal_load_lds_dwordx4 v146, s[40:41] sc1\n\t" \
  "global_load_lds_dwordx4 v146, s[40:41] offset:1024 sc1\n\ts_add_u32 s40, s40, 10240\n\ts_addc_u32 s41, s41, 0\n\ts_branch .Lq%=\n\t.Lp%=:\n\t" \
  "global_load_dwordx4 v[158:161], v[150:151], off\n\tglobal_load_dwordx4 v[162:165], v[152:153], off\n\t" \
  "global_load_dwordx4 v[150:153], v[154:155], off\n\tglobal_load_dwordx4 v[154:157], v[156:157], off\n\t.Lq%=:\n\tv_mov_b32 v109, v108\n\t" \
  "v_mov_b32 v110, v108\n\tv_mov_b32 v111, v108\n\tv_mov_b32 v112, v108\n\tv_mov_b32 v113, v108\n\tv_mov_b32 v114, v108\n\tv_mov_b32 v115, v108\n\t" \
  "v_mov_b32 v116, v108\n\tv_mov_b32 v117, v108\n\tv_mov_b32 v118, v108\n\tv_mov_b32 v119, v108\n\tv_mov_b32 v120, v108\n\tv_mov_b32 v121, v108\n\t" \
  "v_mov_b32 v122, v108\n\tv_mov_b32 v123, v108\n\tv_mov_b32 v125, v124\n\tv_mov_b32 v126, v124\n\tv_mov_b32 v127, v124\n\tv_mov_b32 v128, v124\n\t" \
  "v_mov_b32 v129, v124\n\tv_mov_b32 v130, v124\n\tv_mov_b32 v131, v124\n\tv_mov_b32 v132, v124\n\tv_mov_b32 v133, v124\n\tv_mov_b32 v134, v124\n\t" \
  "v_mov_b32 v135, v124\n\tv_mov_b32 v136, v124\n\tv_mov_b32 v137, v124\n\tv_mov_b32 v138, v124\n\tv_mov_b32 v139, v124\n\tv_add_u32 v143, v142, v145\n\t" \
  "v_add_u32 v144, v143, v145\n\tv_add_u32 v145, v144, v145\n\ts_mov_b32 %[it], 0\n\ts_mov_b32 %[tgt], 8\n\ts_waitcnt lgkmcnt(0)\n\t" \
  "v_mfma_f32_32x32x16_bf16 v[32:47], v[76:79], v[64:67], v[108:123]\n\tv_mfma_f32_32x32x8_bf16 v[32:47], v[80:81], v[68:69], v[32:47]\n\ts_nop 10\n\t" \
  ".Lloop%=:\n\tv_mfma_f32_32x32x16_bf16 v[48:63], v[76:79], v[70:73], v[124:139]\n\tds_read_b128 v[76:79], v140 offset:1024\n\tv_exp_f32 v32, v32\n\t" \
  "v_exp_f32 v33, v33\n\tv_exp_f32 v34, v34\n\tv_exp_f32 v35, v35\n\tv_mfma_f32_32x32x8_bf16 v[48:63], v[80:81], v[74:75], v[48:63]\n\t" \
  "ds_read_b64 v[80:81], v141 offset:256\n\tv_exp_f32 v36, v36\n\tv_exp_f32 v37, v37\n\tv_exp_f32 v38, v38\n\tv_exp_f32 v39, v39\n\t" \
  "v_cvt_pk_bf16_f32 v92, v32, v33\n\tv_cvt_pk_bf16_f32 v93, v34, v35\n\tv_mfma_f32_32x32x16_bf16 v[16:31], v[84:87], v[100:103], v[16:31]\n\t" \
  "ds_read_b128 v[84:87], v142 offset:0\n\tv_exp_f32 v40, v40\n\tv_exp_f32 v41, v41\n\tv_exp_f32 v42, v42\n\tv_exp_f32 v43, v43\n\t" \
  "v_cvt_pk_bf16_f32 v94, v36, v37\n\tv_cvt_pk_bf16_f32 v95, v38, v39\n\tv_mfma_f32_32x32x16_bf16 v[16:31], v[88:91], v[104:107], v[16:31]\n\t" \
  "ds_read_b128 v[88:91], v142 offset:640\n\tv_exp_f32 v44, v44\n\tv_exp_f32 v45, v45\n\tv_exp_f32 v46, v46\n\tv_exp_f32 v47, v47\n\t" \
  "v_cvt_pk_bf16_f32 v96, v40, v41\n\tv_cvt_pk_bf16_f32 v97, v42, v43\n\tv_cvt_pk_bf16_f32 v98, v44, v45\n\tv_cvt_pk_bf16_f32 v99, v46, v47\n\t" \
  "s_cmp_ge_u32 %[dma], 5\n\ts_cbranch_scc1 .Lz0a%=\n\ts_waitcnt vmcnt(2)\n\t.Lz0a%=:\n\ts_mov_b64 exec, 1\n\tds_add_u32 v147, v148\n\t" \
  "s_mov_b64 exec, -1\n\ts_setprio 1\n\ts_waitcnt lgkmcnt(4)\n\tv_mfma_f32_32x32x16_bf16 v[32:47], v[76:79], v[64:67], v[108:123]\n\t" \
  "v_exp_f32 v48, v48\n\tv_exp_f32 v49, v49\n\tv_exp_f32 v50, v50\n\tv_exp_f32 v51, v51\n\ts_waitcnt lgkmcnt(3)\n\t" \
  "v_mfma_f32_32x32x8_bf16 v[32:47], v[80:81], v[68:69], v[32:47]\n\tv_exp_f32 v52, v52\n\tv_exp_f32 v53, v53\n\tv_exp_f32 v54, v54\n\t" \
  "v_exp_f32 v55, v55\n\tv_cvt_pk_bf16_f32 v100, v48, v49\n\tv_cvt_pk_bf16_f32 v101, v50, v51\n\ts_waitcnt lgkmcnt(2)\n\t" \
  "v_mfma_f32_32x32x16_bf16 v[0:15], v[84:87], v[92:95], v[0:15]\n\tv_exp_f32 v56, v56\n\tv_exp_f32 v57, v57\n\tv_exp_f32 v58, v58\n\t" \
  "v_exp_f32 v59, v59\n\tv_cvt_pk_bf16_f32 v102, v52, v53\n\tv_cvt_pk_bf16_f32 v103, v54, v55\n\ts_waitcnt lgkmcnt(1)\n\t" \
  "v_mfma_f32_32x32x16_bf16 v[0:15], v[88:91], v[96:99], v[0:15]\n\tds_read_b32 v149, v147\n\tv_exp_f32 v60, v60\n\tv_exp_f32 v61, v61\n\t" \
  "v_exp_f32 v62, v62\n\tv_exp_f32 v63, v63\n\tv_cvt_pk_bf16_f32 v104, v56, v57\n\tv_cvt_pk_bf16_f32 v105, v58, v59\n\t" \
  "v_cvt_pk_bf16_f32 v106, v60, v61\n\tv_cvt_pk_bf16_f32 v107, v62, v63\n\ts_waitcnt lgkmcnt(0)\n\tv_readfirstlane_b32 %[sv], v149\n\t" \
  "s_cmp_ge_u32 %[sv], %[tgt]\n\ts_cbranch_scc1 .Lx0a%=\n\ts_mov_b32 %[cnt], 0x100000\n\t.Lw0a%=:\n\tds_read_b32 v149, v147\n\ts_waitcnt lgkmcnt(0)\n\t" \
  "v_readfirstlane_b32 %[sv], v149\n\ts_cmp_ge_u32 %[sv], %[tgt]\n\ts_cbranch_scc1 .Lx0a%=\n\ts_sleep 1\n\ts_sub_u32 %[cnt], %[cnt], 1\n\t" \
  "s_cbranch_scc0 .Lw0a%=\n\t.Lx0a%=:\n\ts_add_u32 %[tgt], %[tgt], 8\n\ts_setprio 2\n\ts_cmp_ge_u32 %[dma], 5\n\ts_cbranch_scc1 .Ld0a%=\n\t" \
  "s_add_u32 m0, %[dst], 30720\n\ts_nop 0\n\tglobal_load_lds_dwordx4 v146, s[40:41] sc1\n\t" \
  "global_load_lds_dwordx4 v146, s[40:41] offset:1024 sc1\n\ts_add_u32 s40, s40, 10240\n\ts_addc_u32 s41, s41, 0\n\t.Ld0a%=:\n\t" \
  "v_mfma_f32_32x32x16_bf16 v[48:63], v[76:79], v[70:73], v[124:139]\n\tds_read_b128 v[76:79], v140 offset:10240\n\tv_exp_f32 v32, v32\n\t" \
  "v_exp_f32 v33, v33\n\tv_exp_f32 v34, v34\n\tv_exp_f32 v35, v35\n\tv_mfma_f32_32x32x8_bf16 v[48:63], v[80:81], v[74:75], v[48:63]\n\t" \
  "ds_read_b64 v[80:81], v141 offset:10240\n\tv_exp_f32 v36, v36\n\tv_exp_f32 v37, v37\n\tv_exp_f32 v38, v38\n\tv_exp_f32 v39, v39\n\t" \
  "v_cvt_pk_bf16_f32 v92, v32, v33\n\tv_cvt_pk_bf16_f32 v93, v34, v35\n\tv_mfma_f32_32x32x16_bf16 v[16:31], v[84:87], v[100:103], v[16:31]\n\t" \
  "ds_read_b128 v[84:87], v142 offset:1280\n\tv_exp_f32 v40, v40\n\tv_exp_f32 v41, v41\n\tv_exp_f32 v42, v42\n\tv_exp_f32 v43, v43\n\t" \
  "v_cvt_pk_bf16_f32 v94, v36, v37\n\tv_cvt_pk_bf16_f32 v95, v38, v39\n\tv_mfma_f32_32x32x16_bf16 v[16:31], v[88:91], v[104:107], v[16:31]\n\t" \
  "ds_read_b128 v[88:91], v142 offset:1920\n\tv_exp_f32 v44, v44\n\tv_exp_f32 v45, v45\n\tv_exp_f32 v46, v46\n\tv_exp_f32 v47, v47\n\t" \
  "v_cvt_pk_bf16_f32 v96, v40, v41\n\tv_cvt_pk_bf16_f32 v97, v42, v43\n\tv_cvt_pk_bf16_f32 v98, v44, v45\n\tv_cvt_pk_bf16_f32 v99, v46, v47\n\t" \
  "s_setprio 2\n\ts_waitcnt lgkmcnt(3)\n\tv_mfma_f32_32x32x16_bf16 v[32:47], v[76:79], v[64:67], v[108:123]\n\tv_exp_f32 v48, v48\n\t" \
  "v_exp_f32 v49, v49\n\tv_exp_f32 v50, v50\n\tv_exp_f32 v51, v51\n\ts_waitcnt lgkmcnt(2)\n\t" \
  "v_mfma_f32_32x32x8_bf16 v[32:47], v[80:81], v[68:69], v[32:47]\n\tv_exp_f32 v52, v52\n\tv_exp_f32 v53, v53\n\tv_exp_f32 v54, v54\n\t" \
  "v_exp_f32 v55, v55\n\tv_cvt_pk_bf16_f32 v100, v48, v49\n\tv_cvt_pk_bf16_f32 v101, v50, v51\n\ts_waitcnt lgkmcnt(1)\n\t" \
  "v_mfma_f32_32x32x16_bf16 v[0:15], v[84:87], v[92:95], v[0:15]\n\tv_exp_f32 v56, v56\n\tv_exp_f32 v57, v57\n\tv_exp_f32 v58, v58\n\t" \
  "v_exp_f32 v59, v59\n\tv_cvt_pk_bf16_f32 v102, v52, v53\n\tv_cvt_pk_bf16_f32 v103, v54, v55\n\ts_waitcnt lgkmcnt(0)\n\t" \
  "v_mfma_f32_32x32x16_bf16 v[0:15], v[88:91], v[96:99], v[0:15]\n\tv_exp_f32 v60, v60\n\tv_exp_f32 v61, v61\n\tv_exp_f32 v62, v62\n\t" \
  "v_exp_f32 v63, v63\n\tv_cvt_pk_bf16_f32 v104, v56, v57\n\tv_cvt_pk_bf16_f32 v105, v58, v59\n\tv_cvt_pk_bf16_f32 v106, v60, v61\n\t" \
  "v_cvt_pk_bf16_f32 v107, v62, v63\n\ts_setprio 2\n\tv_mfma_f32_32x32x16_bf16 v[48:63], v[76:79], v[70:73], v[124:139]\n\t" \
  "ds_read_b128 v[76:79], v140 offset:11264\n\tv_exp_f32 v32, v32\n\tv_exp_f32 v33, v33\n\tv_exp_f32 v34, v34\n\tv_exp_f32 v35, v35\n\t" \
  "v_mfma_f32_32x32x8_bf16 v[48:63], v[80:81], v[74:75], v[48:63]\n\tds_read_b64 v[80:81], v141 offset:10496\n\tv_exp_f32 v36, v36\n\t" \
  "v_exp_f32 v37, v37\n\tv_exp_f32 v38, v38\n\tv_exp_f32 v39, v39\n\tv_cvt_pk_bf16_f32 v92, v32, v33\n\tv_cvt_pk_bf16_f32 v93, v34, v35\n\t" \
  "v_mfma_f32_32x32x16_bf16 v[16:31], v[84:87], v[100:103], v[16:31]\n\tds_read_b128 v[84:87], v143 offset:0\n\tv_exp_f32 v40, v40\n\t" \
  "v_exp_f32 v41, v41\n\tv_exp_f32 v42, v42\n\tv_exp_f32 v43, v43\n\tv_cvt_pk_bf16_f32 v94, v36, v37\n\tv_cvt_pk_bf16_f32 v95, v38, v39\n\t" \
  "v_mfma_f32_32x32x16_bf16 v[16:31], v[88:91], v[104:107], v[16:31]\n\tds_read_b128 v[88:91], v143 offset:640\n\tv_exp_f32 v44, v44\n\t" \
  "v_exp_f32 v45, v45\n\tv_exp_f32 v46, v46\n\tv_exp_f32 v47, v47\n\tv_cvt_pk_bf16_f32 v96, v40, v41\n\tv_cvt_pk_bf16_f32 v97, v42, v43\n\t" \
  "v_cvt_pk_bf16_f32 v98, v44, v45\n\tv_cvt_pk_bf16_f32 v99, v46, v47\n\ts_cmp_ge_u32 %[dma], 5\n\ts_cbranch_scc1 .Lz1a%=\n\ts_waitcnt vmcnt(2)\n\t" \
  ".Lz1a%=:\n\ts_mov_b64 exec, 1\n\tds_add_u32 v147, v148\n\ts_mov_b64 exec, -1\n\ts_setprio 1\n\ts_waitcnt lgkmcnt(4)\n\t" \
  "v_mfma_f32_32x32x16_bf16 v[32:47], v[76:79], v[64:67], v[108:123]\n\tv_exp_f32 v48, v48\n\tv_exp_f32 v49, v49\n\tv_exp_f32 v50, v50\n\t" \
  "v_exp_f32 v51, v51\n\ts_waitcnt lgkmcnt(3)\n\tv_mfma_f32_32x32x8_bf16 v[32:47], v[80:81], v[68:69], v[32:47]\n\tv_exp_f32 v52, v52\n\t" \
  "v_exp_f32 v53, v53\n\tv_exp_f32 v54, v54\n\tv_exp_f32 v55, v55\n\tv_cvt_pk_bf16_f32 v100, v48, v49\n\tv_cvt_pk_bf16_f32 v101, v50, v51\n\t" \
  "s_waitcnt lgkmcnt(2)\n\tv_mfma_f32_32x32x16_bf16 v[0:15], v[84:87], v[92:95], v[0:15]\n\tv_exp_f32 v56, v56\n\tv_exp_f32 v57, v57\n\t" \
  "v_exp_f32 v58, v58\n\tv_exp_f32 v59, v59\n\tv_cvt_pk_bf16_f32 v102, v52, v53\n\tv_cvt_pk_bf16_f32 v103, v54, v55\n\ts_waitcnt lgkmcnt(1)\n\t" \
  "v_mfma_f32_32x32x16_bf16 v[0:15], v[88:91], v[96:99], v[0:15]\n\tds_read_b32 v149, v147\n\tv_exp_f32 v60, v60\n\tv_exp_f32 v61, v61\n\t" \
  "v_exp_f32 v62, v62\n\tv_exp_f32 v63, v63\n\tv_cvt_pk_bf16_f32 v104, v56, v57\n\tv_cvt_pk_bf16_f32 v105, v58, v59\n\t" \
  "v_cvt_pk_bf16_f32 v106, v60, v61\n\tv_cvt_pk_bf16_f32 v107, v62, v63\n\ts_waitcnt lgkmcnt(0)\n\tv_readfirstlane_b32 %[sv], v149\n\t" \
  "s_cmp_ge_u32 %[sv], %[tgt]\n\ts_cbranch_scc1 .Lx1a%=\n\ts_mov_b32 %[cnt], 0x100000\n\t.Lw1a%=:\n\tds_read_b32 v149, v147\n\ts_waitcnt lgkmcnt(0)\n\t" \
  "v_readfirstlane_b32 %[sv], v149\n\ts_cmp_ge_u32 %[sv], %[tgt]\n\ts_cbranch_scc1 .Lx1a%=\n\ts_sleep 1\n\ts_sub_u32 %[cnt], %[cnt], 1\n\t" \
  "s_cbranch_scc0 .Lw1a%=\n\t.Lx1a%=:\n\ts_add_u32 %[tgt], %[tgt], 8\n\ts_setprio 2\n\ts_cmp_eq_u32 %[it], 3\n\ts_cbranch_scc1 .Ld1a%=\n\t" \
  "s_cmp_ge_u32 %[dma], 5\n\ts_cbranch_scc1 .Ld1a%=\n\ts_add_u32 m0, %[dst], 0\n\ts_nop 0\n\tglobal_load_lds_dwordx4 v146, s[40:41] sc1\n\t" \
  "global_load_lds_dwordx4 v146, s[40:41] offset:1024 sc1\n\ts_add_u32 s40, s40, 10240\n\ts_addc_u32 s41, s41, 0\n\t.Ld1a%=:\n\t" \
  "v_mfma_f32_32x32x16_bf16 v[48:63], v[76:79], v[70:73], v[124:139]\n\tds_read_b128 v[76:79], v140 offset:20480\n\tv_exp_f32 v32, v32\n\t" \
  "v_exp_f32 v33, v33\n\tv_exp_f32 v34, v34\n\tv_exp_f32 v35, v35\n\tv_mfma_f32_32x32x8_bf16 v[48:63], v[80:81], v[74:75], v[48:63]\n\t" \
  "ds_read_b64 v[80:81], v141 offset:20480\n\tv_exp_f32 v36, v36\n\tv_exp_f32 v37, v37\n\tv_exp_f32 v38, v38\n\tv_exp_f32 v39, v39\n\t" \
  "v_cvt_pk_bf16_f32 v92, v32, v33\n\tv_cvt_pk_bf16_f32 v93, v34, v35\n\tv_mfma_f32_32x32x16_bf16 v[16:31], v[84:87], v[100:103], v[16:31]\n\t" \
  "ds_read_b128 v[84:87], v143 offset:1280\n\tv_exp_f32 v40, v40\n\tv_exp_f32 v41, v41\n\tv_exp_f32 v42, v42\n\tv_exp_f32 v43, v43\n\t" \
  "v_cvt_pk_bf16_f32 v94, v36, v37\n\tv_cvt_pk_bf16_f32 v95, v38, v39\n\tv_mfma_f32_32x32x16_bf16 v[16:31], v[88:91], v[104:107], v[16:31]\n\t" \
  "ds_read_b128 v[88:91], v143 offset:1920\n\tv_exp_f32 v44, v44\n\tv_exp_f32 v45, v45\n\tv_exp_f32 v46, v46\n\tv_exp_f32 v47, v47\n\t" \
  "v_cvt_pk_bf16_f32 v96, v40, v41\n\tv_cvt_pk_bf16_f32 v97, v42, v43\n\tv_cvt_pk_bf16_f32 v98, v44, v45\n\tv_cvt_pk_bf16_f32 v99, v46, v47\n\t" \
  "s_setprio 2\n\ts_waitcnt lgkmcnt(3)\n\tv_mfma_f32_32x32x16_bf16 v[32:47], v[76:79], v[64:67], v[108:123]\n\tv_exp_f32 v48, v48\n\t" \
  "v_exp_f32 v49, v49\n\tv_exp_f32 v50, v50\n\tv_exp_f32 v51, v51\n\ts_waitcnt lgkmcnt(2)\n\t" \
  "v_mfma_f32_32x32x8_bf16 v[32:47], v[80:81], v[68:69], v[32:47]\n\tv_exp_f32 v52, v52\n\tv_exp_f32 v53, v53\n\tv_exp_f32 v54, v54\n\t" \
  "v_exp_f32 v55, v55\n\tv_cvt_pk_bf16_f32 v100, v48, v49\n\tv_cvt_pk_bf16_f32 v101, v50, v51\n\ts_waitcnt lgkmcnt(1)\n\t" \
  "v_mfma_f32_32x32x16_bf16 v[0:15], v[84:87], v[92:95], v[0:15]\n\tv_exp_f32 v56, v56\n\tv_exp_f32 v57, v57\n\tv_exp_f32 v58, v58\n\t" \
  "v_exp_f32 v59, v59\n\tv_cvt_pk_bf16_f32 v102, v52, v53\n\tv_cvt_pk_bf16_f32 v103, v54, v55\n\ts_waitcnt lgkmcnt(0)\n\t" \
  "v_mfma_f32_32x32x16_bf16 v[0:15], v[88:91], v[96:99], v[0:15]\n\tv_exp_f32 v60, v60\n\tv_exp_f32 v61, v61\n\tv_exp_f32 v62, v62\n\t" \
  "v_exp_f32 v63, v63\n\tv_cvt_pk_bf16_f32 v104, v56, v57\n\tv_cvt_pk_bf16_f32 v105, v58, v59\n\tv_cvt_pk_bf16_f32 v106, v60, v61\n\t" \
  "v_cvt_pk_bf16_f32 v107, v62, v63\n\ts_setprio 2\n\tv_mfma_f32_32x32x16_bf16 v[48:63], v[76:79], v[70:73], v[124:139]\n\t" \
  "ds_read_b128 v[76:79], v140 offset:21504\n\tv_exp_f32 v32, v32\n\tv_exp_f32 v33, v33\n\tv_exp_f32 v34, v34\n\tv_exp_f32 v35, v35\n\t" \
  "v_mfma_f32_32x32x8_bf16 v[48:63], v[80:81], v[74:75], v[48:63]\n\tds_read_b64 v[80:81], v141 offset:20736\n\tv_exp_f32 v36, v36\n\t" \
  "v_exp_f32 v37, v37\n\tv_exp_f32 v38, v38\n\tv_exp_f32 v39, v39\n\tv_cvt_pk_bf16_f32 v92, v32, v33\n\tv_cvt_pk_bf16_f32 v93, v34, v35\n\t" \
  "v_mfma_f32_32x32x16_bf16 v[16:31], v[84:87], v[100:103], v[16:31]\n\tds_read_b128 v[84:87], v144 offset:0\n\tv_exp_f32 v40, v40\n\t" \
  "v_exp_f32 v41, v41\n\tv_exp_f32 v42, v42\n\tv_exp_f32 v43, v43\n\tv_cvt_pk_bf16_f32 v94, v36, v37\n\tv_cvt_pk_bf16_f32 v95, v38, v39\n\t" \
  "v_mfma_f32_32x32x16_bf16 v[16:31], v[88:91], v[104:107], v[16:31]\n\tds_read_b128 v[88:91], v144 offset:640\n\tv_exp_f32 v44, v44\n\t" \
  "v_exp_f32 v45, v45\n\tv_exp_f32 v46, v46\n\tv_exp_f32 v47, v47\n\tv_cvt_pk_bf16_f32 v96, v40, v41\n\tv_cvt_pk_bf16_f32 v97, v42, v43\n\t" \
  "v_cvt_pk_bf16_f32 v98, v44, v45\n\tv_cvt_pk_bf16_f32 v99, v46, v47\n\ts_cmp_ge_u32 %[dma], 5\n\ts_cbranch_scc1 .Lz2a%=\n\ts_cmp_lg_u32 %[it], 3\n\t" \
  "s_cbranch_scc1 .Lv2a%=\n\ts_waitcnt vmcnt(0)\n\t.Lv2a%=:\n\ts_waitcnt vmcnt(2)\n\t.Ly2a%=:\n\ts_mov_b64 exec, 1\n\tds_add_u32 v147, v148\n\t" \
  "s_mov_b64 exec, -1\n\ts_setprio 1\n\ts_waitcnt lgkmcnt(4)\n\tv_mfma_f32_32x32x16_bf16 v[32:47], v[76:79], v[64:67], v[108:123]\n\t" \
  "v_exp_f32 v48, v48\n\tv_exp_f32 v49, v49\n\tv_exp_f32 v50, v50\n\tv_exp_f32 v51, v51\n\ts_waitcnt lgkmcnt(3)\n\t" \
  "v_mfma_f32_32x32x8_bf16 v[32:47], v[80:81], v[68:69], v[32:47]\n\tv_exp_f32 v52, v52\n\tv_exp_f32 v53, v53\n\tv_exp_f32 v54, v54\n\t" \
  "v_exp_f32 v55, v55\n\tv_cvt_pk_bf16_f32 v100, v48, v49\n\tv_cvt_pk_bf16_f32 v101, v50, v51\n\ts_waitcnt lgkmcnt(2)\n\t" \
  "v_mfma_f32_32x32x16_bf16 v[0:15], v[84:87], v[92:95], v[0:15]\n\tv_exp_f32 v56, v56\n\tv_exp_f32 v57, v57\n\tv_exp_f32 v58, v58\n\t" \
  "v_exp_f32 v59, v59\n\tv_cvt_pk_bf16_f32 v102, v52, v53\n\tv_cvt_pk_bf16_f32 v103, v54, v55\n\ts_waitcnt lgkmcnt(1)\n\t" \
  "v_mfma_f32_32x32x16_bf16 v[0:15], v[88:91], v[96:99], v[0:15]\n\tds_read_b32 v149, v147\n\tv_exp_f32 v60, v60\n\tv_exp_f32 v61, v61\n\t" \
  "v_exp_f32 v62, v62\n\tv_exp_f32 v63, v63\n\tv_cvt_pk_bf16_f32 v104, v56, v57\n\tv_cvt_pk_bf16_f32 v105, v58, v59\n\t" \
  "v_cvt_pk_bf16_f32 v106, v60, v61\n\tv_cvt_pk_bf16_f32 v107, v62, v63\n\ts_waitcnt lgkmcnt(0)\n\tv_readfirstlane_b32 %[sv], v149\n\t" \
  "s_cmp_ge_u32 %[sv], %[tgt]\n\ts_cbranch_scc1 .Lx2a%=\n\ts_mov_b32 %[cnt], 0x100000\n\t.Lw2a%=:\n\tds_read_b32 v149, v147\n\ts_waitcnt lgkmcnt(0)\n\t" \
  "v_readfirstlane_b32 %[sv], v149\n\ts_cmp_ge_u32 %[sv], %[tgt]\n\ts_cbranch_scc1 .Lx2a%=\n\ts_sleep 1\n\ts_sub_u32 %[cnt], %[cnt], 1\n\t" \
  "s_cbranch_scc0 .Lw2a%=\n\t.Lx2a%=:\n\ts_add_u32 %[tgt], %[tgt], 8\n\ts_setprio 2\n\ts_lshl_b32 %[sv], %[dma], 4\n\t" \
  "s_add_u32 %[sv], %[sv], %[it]\n\ts_cmp_eq_u32 %[sv], 0x71\n\ts_cbranch_scc1 .Ltf%=\n\t.Ltg%=:\n\ts_cmp_eq_u32 %[it], 3\n\ts_cbranch_scc1 .Ld2a%=\n\t" \
  "s_cmp_ge_u32 %[dma], 5\n\ts_cbranch_scc1 .Ld2a%=\n\ts_add_u32 m0, %[dst], 10240\n\ts_nop 0\n\tglobal_load_lds_dwordx4 v146, s[40:41] sc1\n\t" \
  "global_load_lds_dwordx4 v146, s[40:41] offset:1024 sc1\n\ts_add_u32 s40, s40, 10240\n\ts_addc_u32 s41, s41, 0\n\t.Ld2a%=:\n\t" \
  "v_mfma_f32_32x32x16_bf16 v[48:63], v[76:79], v[70:73], v[124:139]\n\tds_read_b128 v[76:79], v140 offset:30720\n\tv_exp_f32 v32, v32\n\t" \
  "v_exp_f32 v33, v33\n\tv_exp_f32 v34, v34\n\tv_exp_f32 v35, v35\n\tv_mfma_f32_32x32x8_bf16 v[48:63], v[80:81], v[74:75], v[48:63]\n\t" \
  "ds_read_b64 v[80:81], v141 offset:30720\n\tv_exp_f32 v36, v36\n\tv_exp_f32 v37, v37\n\tv_exp_f32 v38, v38\n\tv_exp_f32 v39, v39\n\t" \
  "v_cvt_pk_bf16_f32 v92, v32, v33\n\tv_cvt_pk_bf16_f32 v93, v34, v35\n\tv_mfma_f32_32x32x16_bf16 v[16:31], v[84:87], v[100:103], v[16:31]\n\t" \
  "ds_read_b128 v[84:87], v144 offset:1280\n\tv_exp_f32 v40, v40\n\tv_exp_f32 v41, v41\n\tv_exp_f32 v42, v42\n\tv_exp_f32 v43, v43\n\t" \
  "v_cvt_pk_bf16_f32 v94, v36, v37\n\tv_cvt_pk_bf16_f32 v95, v38, v39\n\tv_mfma_f32_32x32x16_bf16 v[16:31], v[88:91], v[104:107], v[16:31]\n\t" \
  "ds_read_b128 v[88:91], v144 offset:1920\n\tv_exp_f32 v44, v44\n\tv_exp_f32 v45, v45\n\tv_exp_f32 v46, v46\n\tv_exp_f32 v47, v47\n\t" \
  "v_cvt_pk_bf16_f32 v96, v40, v41\n\tv_cvt_pk_bf16_f32 v97, v42, v43\n\tv_cvt_pk_bf16_f32 v98, v44, v45\n\tv_cvt_pk_bf16_f32 v99, v46, v47\n\t" \
  "s_setprio 2\n\ts_waitcnt lgkmcnt(3)\n\tv_mfma_f32_32x32x16_bf16 v[32:47], v[76:79], v[64:67], v[108:123]\n\tv_exp_f32 v48, v48\n\t" \
  "v_exp_f32 v49, v49\n\tv_exp_f32 v50, v50\n\tv_exp_f32 v51, v51\n\ts_waitcnt lgkmcnt(2)\n\t" \
  "v_mfma_f32_32x32x8_bf16 v[32:47], v[80:81], v[68:69], v[32:47]\n\tv_exp_f32 v52, v52\n\tv_exp_f32 v53, v53\n\tv_exp_f32 v54, v54\n\t" \
  "v_exp_f32 v55, v55\n\tv_cvt_pk_bf16_f32 v100, v48, v49\n\tv_cvt_pk_bf16_f32 v101, v50, v51\n\ts_waitcnt lgkmcnt(1)\n\t" \
  "v_mfma_f32_32x32x16_bf16 v[0:15], v[84:87], v[92:95], v[0:15]\n\tv_exp_f32 v56, v56\n\tv_exp_f32 v57, v57\n\tv_exp_f32 v58, v58\n\t" \
  "v_exp_f32 v59, v59\n\tv_cvt_pk_bf16_f32 v102, v52, v53\n\tv_cvt_pk_bf16_f32 v103, v54, v55\n\ts_waitcnt lgkmcnt(0)\n\t" \
  "v_mfma_f32_32x32x16_bf16 v[0:15], v[88:91], v[96:99], v[0:15]\n\tv_exp_f32 v60, v60\n\tv_exp_f32 v61, v61\n\tv_exp_f32 v62, v62\n\t" \
  "v_exp_f32 v63, v63\n\tv_cvt_pk_bf16_f32 v104, v56, v57\n\tv_cvt_pk_bf16_f32 v105, v58, v59\n\tv_cvt_pk_bf16_f32 v106, v60, v61\n\t" \
  "v_cvt_pk_bf16_f32 v107, v62, v63\n\ts_setprio 2\n\tv_mfma_f32_32x32x16_bf16 v[48:63], v[76:79], v[70:73], v[124:139]\n\t" \
  "ds_read_b128 v[76:79], v140 offset:31744\n\tv_exp_f32 v32, v32\n\tv_exp_f32 v33, v33\n\tv_exp_f32 v34, v34\n\tv_exp_f32 v35, v35\n\t" \
  "v_mfma_f32_32x32x8_bf16 v[48:63], v[80:81], v[74:75], v[48:63]\n\tds_read_b64 v[80:81], v141 offset:30976\n\tv_exp_f32 v36, v36\n\t" \
  "v_exp_f32 v37, v37\n\tv_exp_f32 v38, v38\n\tv_exp_f32 v39, v39\n\tv_cvt_pk_bf16_f32 v92, v32, v33\n\tv_cvt_pk_bf16_f32 v93, v34, v35\n\t" \
  "v_mfma_f32_32x32x16_bf16 v[16:31], v[84:87], v[100:103], v[16:31]\n\tds_read_b128 v[84:87], v145 offset:0\n\tv_exp_f32 v40, v40\n\t" \
  "v_exp_f32 v41, v41\n\tv_exp_f32 v42, v42\n\tv_exp_f32 v43, v43\n\tv_cvt_pk_bf16_f32 v94, v36, v37\n\tv_cvt_pk_bf16_f32 v95, v38, v39\n\t" \
  "v_mfma_f32_32x32x16_bf16 v[16:31], v[88:91], v[104:107], v[16:31]\n\tds_read_b128 v[88:91], v145 offset:640\n\tv_exp_f32 v44, v44\n\t" \
  "v_exp_f32 v45, v45\n\tv_exp_f32 v46, v46\n\tv_exp_f32 v47, v47\n\tv_cvt_pk_bf16_f32 v96, v40, v41\n\tv_cvt_pk_bf16_f32 v97, v42, v43\n\t" \
  "v_cvt_pk_bf16_f32 v98, v44, v45\n\tv_cvt_pk_bf16_f32 v99, v46, v47\n\ts_cmp_ge_u32 %[dma], 5\n\ts_cbranch_scc1 .Lz3a%=\n\ts_cmp_lg_u32 %[it], 3\n\t" \
  "s_cbranch_scc1 .Lv3a%=\n\ts_waitcnt vmcnt(0)\n\t.Lv3a%=:\n\ts_waitcnt vmcnt(2)\n\t.Lz3a%=:\n\ts_mov_b64 exec, 1\n\tds_add_u32 v147, v148\n\t" \
  "s_mov_b64 exec, -1\n\ts_setprio 1\n\ts_waitcnt lgkmcnt(4)\n\tv_mfma_f32_32x32x16_bf16 v[32:47], v[76:79], v[64:67], v[108:123]\n\t" \
  "v_exp_f32 v48, v48\n\tv_exp_f32 v49, v49\n\tv_exp_f32 v50, v50\n\tv_exp_f32 v51, v51\n\ts_waitcnt lgkmcnt(3)\n\t" \
  "v_mfma_f32_32x32x8_bf16 v[32:47], v[80:81], v[68:69], v[32:47]\n\tv_exp_f32 v52, v52\n\tv_exp_f32 v53, v53\n\tv_exp_f32 v54, v54\n\t" \
  "v_exp_f32 v55, v55\n\tv_cvt_pk_bf16_f32 v100, v48, v49\n\tv_cvt_pk_bf16_f32 v101, v50, v51\n\ts_waitcnt lgkmcnt(2)\n\t" \
  "v_mfma_f32_32x32x16_bf16 v[0:15], v[84:87], v[92:95], v[0:15]\n\tv_exp_f32 v56, v56\n\tv_exp_f32 v57, v57\n\tv_exp_f32 v58, v58\n\t" \
  "v_exp_f32 v59, v59\n\tv_cvt_pk_bf16_f32 v102, v52, v53\n\tv_cvt_pk_bf16_f32 v103, v54, v55\n\ts_waitcnt lgkmcnt(1)\n\t" \
  "v_mfma_f32_32x32x16_bf16 v[0:15], v[88:91], v[96:99], v[0:15]\n\tds_read_b32 v149, v147\n\tv_exp_f32 v60, v60\n\tv_exp_f32 v61, v61\n\t" \
  "v_exp_f32 v62, v62\n\tv_exp_f32 v63, v63\n\tv_cvt_pk_bf16_f32 v104, v56, v57\n\tv_cvt_pk_bf16_f32 v105, v58, v59\n\t" \
  "v_cvt_pk_bf16_f32 v106, v60, v61\n\tv_cvt_pk_bf16_f32 v107, v62, v63\n\ts_waitcnt lgkmcnt(0)\n\tv_readfirstlane_b32 %[sv], v149\n\t" \
  "s_cmp_ge_u32 %[sv], %[tgt]\n\ts_cbranch_scc1 .Lx3a%=\n\ts_mov_b32 %[cnt], 0x100000\n\t.Lw3a%=:\n\tds_read_b32 v149, v147\n\ts_waitcnt lgkmcnt(0)\n\t" \
  "v_readfirstlane_b32 %[sv], v149\n\ts_cmp_ge_u32 %[sv], %[tgt]\n\ts_cbranch_scc1 .Lx3a%=\n\ts_sleep 1\n\ts_sub_u32 %[cnt], %[cnt], 1\n\t" \
  "s_cbranch_scc0 .Lw3a%=\n\t.Lx3a%=:\n\ts_add_u32 %[tgt], %[tgt], 8\n\ts_setprio 2\n\ts_cmp_eq_u32 %[it], 3\n\ts_cbranch_scc1 .Ld3a%=\n\t" \
  "s_cmp_ge_u32 %[dma], 5\n\ts_cbranch_scc1 .Ld3a%=\n\ts_add_u32 m0, %[dst], 20480\n\ts_nop 0\n\tglobal_load_lds_dwordx4 v146, s[40:41] sc1\n\t" \
  "global_load_lds_dwordx4 v146, s[40:41] offset:1024 sc1\n\ts_add_u32 s40, s40, 10240\n\ts_addc_u32 s41, s41, 0\n\t.Ld3a%=:\n\t" \
  "v_mfma_f32_32x32x16_bf16 v[48:63], v[76:79], v[70:73], v[124:139]\n\tds_read_b128 v[76:79], v140 offset:0\n\tv_exp_f32 v32, v32\n\t" \
  "v_exp_f32 v33, v33\n\tv_exp_f32 v34, v34\n\tv_exp_f32 v35, v35\n\tv_mfma_f32_32x32x8_bf16 v[48:63], v[80:81], v[74:75], v[48:63]\n\t" \
  "ds_read_b64 v[80:81], v141 offset:0\n\tv_exp_f32 v36, v36\n\tv_exp_f32 v37, v37\n\tv_exp_f32 v38, v38\n\tv_exp_f32 v39, v39\n\t" \
  "v_cvt_pk_bf16_f32 v92, v32, v33\n\tv_cvt_pk_bf16_f32 v93, v34, v35\n\tv_mfma_f32_32x32x16_bf16 v[16:31], v[84:87], v[100:103], v[16:31]\n\t" \
  "ds_read_b128 v[84:87], v145 offset:1280\n\tv_exp_f32 v40, v40\n\tv_exp_f32 v41, v41\n\tv_exp_f32 v42, v42\n\tv_exp_f32 v43, v43\n\t" \
  "v_cvt_pk_bf16_f32 v94, v36, v37\n\tv_cvt_pk_bf16_f32 v95, v38, v39\n\tv_mfma_f32_32x32x16_bf16 v[16:31], v[88:91], v[104:107], v[16:31]\n\t" \
  "ds_read_b128 v[88:91], v145 offset:1920\n\tv_exp_f32 v44, v44\n\tv_exp_f32 v45, v45\n\tv_exp_f32 v46, v46\n\tv_exp_f32 v47, v47\n\t" \
  "v_cvt_pk_bf16_f32 v96, v40, v41\n\tv_cvt_pk_bf16_f32 v97, v42, v43\n\tv_cvt_pk_bf16_f32 v98, v44, v45\n\tv_cvt_pk_bf16_f32 v99, v46, v47\n\t" \
  "s_setprio 2\n\ts_waitcnt lgkmcnt(3)\n\tv_mfma_f32_32x32x16_bf16 v[32:47], v[76:79], v[64:67], v[108:123]\n\tv_exp_f32 v48, v48\n\t" \
  "v_exp_f32 v49, v49\n\tv_exp_f32 v50, v50\n\tv_exp_f32 v51, v51\n\ts_waitcnt lgkmcnt(2)\n\t" \
  "v_mfma_f32_32x32x8_bf16 v[32:47], v[80:81], v[68:69], v[32:47]\n\tv_exp_f32 v52, v52\n\tv_exp_f32 v53, v53\n\tv_exp_f32 v54, v54\n\t" \
  "v_exp_f32 v55, v55\n\tv_cvt_pk_bf16_f32 v100, v48, v49\n\tv_cvt_pk_bf16_f32 v101, v50, v51\n\ts_waitcnt lgkmcnt(1)\n\t" \
  "v_mfma_f32_32x32x16_bf16 v[0:15], v[84:87], v[92:95], v[0:15]\n\tv_exp_f32 v56, v56\n\tv_exp_f32 v57, v57\n\tv_exp_f32 v58, v58\n\t" \
  "v_exp_f32 v59, v59\n\tv_cvt_pk_bf16_f32 v102, v52, v53\n\tv_cvt_pk_bf16_f32 v103, v54, v55\n\ts_waitcnt lgkmcnt(0)\n\t" \
  "v_mfma_f32_32x32x16_bf16 v[0:15], v[88:91], v[96:99], v[0:15]\n\tv_exp_f32 v60, v60\n\tv_exp_f32 v61, v61\n\tv_exp_f32 v62, v62\n\t" \
  "v_exp_f32 v63, v63\n\tv_cvt_pk_bf16_f32 v104, v56, v57\n\tv_cvt_pk_bf16_f32 v105, v58, v59\n\tv_cvt_pk_bf16_f32 v106, v60, v61\n\t" \
  "v_cvt_pk_bf16_f32 v107, v62, v63\n\ts_setprio 2\n\ts_add_u32 %[it], %[it], 1\n\ts_cmp_lg_u32 %[it], 4\n\ts_cbranch_scc1 .Lloop%=\n\t" \
  "s_branch .Lend%=\n\t.Lz2a%=:\n\ts_cmp_gt_u32 %[it], 1\n\ts_cbranch_scc1 .Ly2a%=\n\ts_waitcnt vmcnt(0)\n\ts_cmp_eq_u32 %[it], 1\n\t" \
  "s_cbranch_scc1 .Ly2a%=\n\tglobal_store_dwordx4 v146, v[158:161], %[w0] sc1\n\tglobal_store_dwordx4 v146, v[162:165], %[w1] sc1\n\t" \
  "global_store_dwordx4 v146, v[150:153], %[w2] sc1\n\ts_cmp_ge_u32 %[pc], 52\n\ts_cbranch_scc1 .Lo%=\n\t" \
  "global_store_dwordx4 v146, v[154:157], %[w3] sc1\n\t.Lo%=:\n\ts_branch .Ly2a%=\n\t.Ltf%=:\n\ts_mov_b64 exec, 1\n\t" \
  "global_store_dword v82, v83, %[tf] sc1\n\ts_mov_b64 exec, -1\n\ts_branch .Ltg%=\n\t.Lend%=:\n\t" \
  "v_mfma_f32_32x32x16_bf16 v[16:31], v[84:87], v[100:103], v[16:31]\n\tv_mfma_f32_32x32x16_bf16 v[16:31], v[88:91], v[104:107], v[16:31]\n\ts_nop 15\n\t"
#define ATTN_CLOB "v32","v33","v34","v35","v36","v37","v38","v39","v40","v41","v42","v43","v44","v45","v46","v47","v48","v49","v50","v51","v52","v53","v54","v55","v56","v57","v58","v59","v60","v61","v62","v63","v76","v77","v78","v79","v80","v81","v92","v93","v94","v95","v96","v97","v98","v99","v109","v110","v111","v112","v113","v114","v115","v116","v117","v118","v119","v120","v121","v122","v123","v125","v126","v127","v128","v129","v130","v131","v132","v133","v134","v135","v136","v137","v138","v139","v143","v144","v158","v159","v160","v161","v162","v163","v164","v165"
#define UP_LOOP \
  "ds_read_b128 v[96:99], v136 offset:0\n\tds_read_b128 v[100:103], v136 offset:4096\n\tds_read_b64_tr_b16 v[112:113], v140 offset:0\n\t" \
  "ds_read_b64_tr_b16 v[114:115], v140 offset:768\n\tds_read_b64_tr_b16 v[116:117], v140 offset:32\n\tds_read_b64_tr_b16 v[118:119], v140 offset:800\n\t" \
  "ds_read_b64_tr_b16 v[120:121], v140 offset:64\n\tds_read_b64_tr_b16 v[122:123], v140 offset:832\n\t.Lul%=:\n\ts_waitcnt lgkmcnt(0)\n\ts_setprio 0\n\t" \
  "v_mfma_f32_32x32x16_bf16 v[0:15], v[112:115], v[96:99], v[0:15]\n\ts_setprio 3\n\tds_read_b128 v[104:107], v137 offset:0\n\t" \
  "ds_read_b128 v[108:111], v137 offset:4096\n\ts_setprio 0\n\tv_mfma_f32_32x32x16_bf16 v[16:31], v[112:115], v[100:103], v[16:31]\n\ts_setprio 3\n\t" \
  "ds_read_b64_tr_b16 v[124:125], v140 offset:3072\n\tds_read_b64_tr_b16 v[126:127], v140 offset:3840\n\ts_setprio 0\n\t" \
  "v_mfma_f32_32x32x16_bf16 v[32:47], v[116:119], v[96:99], v[32:47]\n\ts_setprio 3\n\tds_read_b64_tr_b16 v[128:129], v140 offset:3104\n\t" \
  "ds_read_b64_tr_b16 v[130:131], v140 offset:3872\n\ts_setprio 0\n\tv_mfma_f32_32x32x16_bf16 v[48:63], v[116:119], v[100:103], v[48:63]\n\t" \
  "s_setprio 3\n\tds_read_b64_tr_b16 v[132:133], v140 offset:3136\n\tds_read_b64_tr_b16 v[134:135], v140 offset:3904\n\ts_setprio 0\n\t" \
  "v_mfma_f32_32x32x16_bf16 v[64:79], v[120:123], v[96:99], v[64:79]\n\ts_setprio 3\n\ts_mov_b32 m0, %[w2]\n\ts_nop 0\n\t" \
  "global_load_lds_dwordx4 v159, s[42:43]\n\ts_setprio 0\n\tv_mfma_f32_32x32x16_bf16 v[80:95], v[120:123], v[100:103], v[80:95]\n\ts_setprio 3\n\t" \
  "global_load_lds_dwordx4 v160, s[42:43] offset:1024\n\tglobal_load_lds_dwordx4 v161, s[42:43] offset:2048\n\ts_waitcnt lgkmcnt(0)\n\ts_setprio 0\n\t" \
  "v_mfma_f32_32x32x16_bf16 v[0:15], v[124:127], v[104:107], v[0:15]\n\ts_setprio 3\n\tds_read_b128 v[96:99], v138 offset:0\n\t" \
  "ds_read_b128 v[100:103], v138 offset:4096\n\ts_setprio 0\n\tv_mfma_f32_32x32x16_bf16 v[16:31], v[124:127], v[108:111], v[16:31]\n\ts_setprio 3\n\t" \
  "ds_read_b64_tr_b16 v[112:113], v140 offset:6144\n\tds_read_b64_tr_b16 v[114:115], v140 offset:6912\n\ts_setprio 0\n\t" \
  "v_mfma_f32_32x32x16_bf16 v[32:47], v[128:131], v[104:107], v[32:47]\n\ts_setprio 3\n\tds_read_b64_tr_b16 v[116:117], v140 offset:6176\n\t" \
  "ds_read_b64_tr_b16 v[118:119], v140 offset:6944\n\ts_setprio 0\n\tv_mfma_f32_32x32x16_bf16 v[48:63], v[128:131], v[108:111], v[48:63]\n\t" \
  "s_setprio 3\n\tds_read_b64_tr_b16 v[120:121], v140 offset:6208\n\tds_read_b64_tr_b16 v[122:123], v140 offset:6976\n\ts_add_u32 s42, s42, 196608\n\t" \
  "s_addc_u32 s43, s43, 0\n\ts_setprio 0\n\tv_mfma_f32_32x32x16_bf16 v[64:79], v[132:135], v[104:107], v[64:79]\n\ts_setprio 3\n\t" \
  "s_add_u32 m0, %[w2], 12320\n\ts_nop 0\n\tglobal_load_lds_dwordx4 v159, s[44:45]\n\ts_setprio 0\n\t" \
  "v_mfma_f32_32x32x16_bf16 v[80:95], v[132:135], v[108:111], v[80:95]\n\ts_setprio 3\n\tglobal_load_lds_dwordx4 v160, s[44:45] offset:1024\n\t" \
  "global_load_lds_dwordx4 v161, s[44:45] offset:2048\n\ts_waitcnt lgkmcnt(0)\n\ts_setprio 0\n\t" \
  "v_mfma_f32_32x32x16_bf16 v[0:15], v[112:115], v[96:99], v[0:15]\n\ts_setprio 3\n\tds_read_b128 v[104:107], v139 offset:0\n\t" \
  "ds_read_b128 v[108:111], v139 offset:4096\n\tds_read_b64_tr_b16 v[124:125], v140 offset:9216\n\tds_read_b64_tr_b16 v[126:127], v140 offset:9984\n\t" \
  "s_setprio 0\n\tv_mfma_f32_32x32x16_bf16 v[16:31], v[112:115], v[100:103], v[16:31]\n\ts_setprio 3\n\t" \
  "ds_read_b64_tr_b16 v[128:129], v140 offset:9248\n\tds_read_b64_tr_b16 v[130:131], v140 offset:10016\n\t" \
  "ds_read_b64_tr_b16 v[132:133], v140 offset:9280\n\tds_read_b64_tr_b16 v[134:135], v140 offset:10048\n\ts_setprio 0\n\t" \
  "v_mfma_f32_32x32x16_bf16 v[32:47], v[116:119], v[96:99], v[32:47]\n\ts_setprio 3\n\ts_waitcnt vmcnt(6)\n\tds_add_u32 v153, v151\n\ts_setprio 0\n\t" \
  "v_mfma_f32_32x32x16_bf16 v[48:63], v[116:119], v[100:103], v[48:63]\n\ts_setprio 3\n\ts_add_u32 s44, s44, 196608\n\ts_addc_u32 s45, s45, 0\n\t" \
  "s_setprio 0\n\tv_mfma_f32_32x32x16_bf16 v[64:79], v[120:123], v[96:99], v[64:79]\n\ts_setprio 3\n\ts_add_u32 %[tgt], %[tgt], 4\n\ts_setprio 0\n\t" \
  "v_mfma_f32_32x32x16_bf16 v[80:95], v[120:123], v[100:103], v[80:95]\n\ts_setprio 3\n\tds_read_b32 v152, v150\n\ts_waitcnt lgkmcnt(0)\n\t" \
  "v_readfirstlane_b32 %[t0], v152\n\ts_cmp_lt_u32 %[t0], %[tgt]\n\ts_cbranch_scc1 .Lw0u%=\n\t.Lx0u%=:\n\ts_setprio 0\n\t" \
  "v_mfma_f32_32x32x16_bf16 v[0:15], v[124:127], v[104:107], v[0:15]\n\ts_setprio 3\n\tds_read_b128 v[96:99], v136 offset:45056\n\t" \
  "ds_read_b128 v[100:103], v136 offset:49152\n\ts_setprio 0\n\tv_mfma_f32_32x32x16_bf16 v[16:31], v[124:127], v[108:111], v[16:31]\n\ts_setprio 3\n\t" \
  "ds_read_b64_tr_b16 v[112:113], v141 offset:0\n\tds_read_b64_tr_b16 v[114:115], v141 offset:768\n\ts_setprio 0\n\t" \
  "v_mfma_f32_32x32x16_bf16 v[32:47], v[128:131], v[104:107], v[32:47]\n\ts_setprio 3\n\tds_read_b64_tr_b16 v[116:117], v141 offset:32\n\t" \
  "ds_read_b64_tr_b16 v[118:119], v141 offset:800\n\ts_setprio 0\n\tv_mfma_f32_32x32x16_bf16 v[48:63], v[128:131], v[108:111], v[48:63]\n\t" \
  "s_setprio 3\n\tds_read_b64_tr_b16 v[120:121], v141 offset:64\n\tds_read_b64_tr_b16 v[122:123], v141 offset:832\n\ts_add_u32 m0, %[xd], 0\n\t" \
  "s_setprio 0\n\tv_mfma_f32_32x32x16_bf16 v[64:79], v[132:135], v[104:107], v[64:79]\n\ts_setprio 3\n\tglobal_load_lds_dwordx4 v144, s[46:47]\n\t" \
  "global_load_lds_dwordx4 v145, s[46:47] offset:1024\n\ts_setprio 0\n\tv_mfma_f32_32x32x16_bf16 v[80:95], v[132:135], v[108:111], v[80:95]\n\t" \
  "s_setprio 3\n\tglobal_load_lds_dwordx4 v146, s[46:47] offset:2048\n\tglobal_load_lds_dwordx4 v147, s[46:47] offset:3072\n\ts_add_u32 s46, s46, 128\n\t" \
  "s_addc_u32 s47, s47, 0\n\ts_waitcnt lgkmcnt(0)\n\ts_setprio 0\n\tv_mfma_f32_32x32x16_bf16 v[0:15], v[112:115], v[96:99], v[0:15]\n\ts_setprio 3\n\t" \
  "ds_read_b128 v[104:107], v137 offset:45056\n\tds_read_b128 v[108:111], v137 offset:49152\n\ts_setprio 0\n\t" \
  "v_mfma_f32_32x32x16_bf16 v[16:31], v[112:115], v[100:103], v[16:31]\n\ts_setprio 3\n\tds_read_b64_tr_b16 v[124:125], v141 offset:3072\n\t" \
  "ds_read_b64_tr_b16 v[126:127], v141 offset:3840\n\ts_setprio 0\n\tv_mfma_f32_32x32x16_bf16 v[32:47], v[116:119], v[96:99], v[32:47]\n\ts_setprio 3\n\t" \
  "ds_read_b64_tr_b16 v[128:129], v141 offset:3104\n\tds_read_b64_tr_b16 v[130:131], v141 offset:3872\n\ts_setprio 0\n\t" \
  "v_mfma_f32_32x32x16_bf16 v[48:63], v[116:119], v[100:103], v[48:63]\n\ts_setprio 3\n\tds_read_b64_tr_b16 v[132:133], v141 offset:3136\n\t" \
  "ds_read_b64_tr_b16 v[134:135], v141 offset:3904\n\ts_setprio 0\n\tv_mfma_f32_32x32x16_bf16 v[64:79], v[120:123], v[96:99], v[64:79]\n\ts_setprio 3\n\t" \
  "s_mov_b32 m0, %[w0]\n\ts_nop 0\n\tglobal_load_lds_dwordx4 v159, s[42:43]\n\ts_setprio 0\n\t" \
  "v_mfma_f32_32x32x16_bf16 v[80:95], v[120:123], v[100:103], v[80:95]\n\ts_setprio 3\n\tglobal_load_lds_dwordx4 v160, s[42:43] offset:1024\n\t" \
  "global_load_lds_dwordx4 v161, s[42:43] offset:2048\n\ts_waitcnt lgkmcnt(0)\n\ts_setprio 0\n\t" \
  "v_mfma_f32_32x32x16_bf16 v[0:15], v[124:127], v[104:107], v[0:15]\n\ts_setprio 3\n\tds_read_b128 v[96:99], v138 offset:45056\n\t" \
  "ds_read_b128 v[100:103], v138 offset:49152\n\ts_setprio 0\n\tv_mfma_f32_32x32x16_bf16 v[16:31], v[124:127], v[108:111], v[16:31]\n\ts_setprio 3\n\t" \
  "ds_read_b64_tr_b16 v[112:113], v141 offset:6144\n\tds_read_b64_tr_b16 v[114:115], v141 offset:6912\n\ts_setprio 0\n\t" \
  "v_mfma_f32_32x32x16_bf16 v[32:47], v[128:131], v[104:107], v[32:47]\n\ts_setprio 3\n\tds_read_b64_tr_b16 v[116:117], v141 offset:6176\n\t" \
  "ds_read_b64_tr_b16 v[118:119], v141 offset:6944\n\ts_setprio 0\n\tv_mfma_f32_32x32x16_bf16 v[48:63], v[128:131], v[108:111], v[48:63]\n\t" \
  "s_setprio 3\n\tds_read_b64_tr_b16 v[120:121], v141 offset:6208\n\tds_read_b64_tr_b16 v[122:123], v141 offset:6976\n\ts_add_u32 s42, s42, 196608\n\t" \
  "s_addc_u32 s43, s43, 0\n\ts_setprio 0\n\tv_mfma_f32_32x32x16_bf16 v[64:79], v[132:135], v[104:107], v[64:79]\n\ts_setprio 3\n\t" \
  "s_add_u32 m0, %[w0], 12320\n\ts_nop 0\n\tglobal_load_lds_dwordx4 v159, s[44:45]\n\ts_setprio 0\n\t" \
  "v_mfma_f32_32x32x16_bf16 v[80:95], v[132:135], v[108:111], v[80:95]\n\ts_setprio 3\n\tglobal_load_lds_dwordx4 v160, s[44:45] offset:1024\n\t" \
  "global_load_lds_dwordx4 v161, s[44:45] offset:2048\n\ts_waitcnt lgkmcnt(0)\n\ts_setprio 0\n\t" \
  "v_mfma_f32_32x32x16_bf16 v[0:15], v[112:115], v[96:99], v[0:15]\n\ts_setprio 3\n\tds_read_b128 v[104:107], v139 offset:45056\n\t" \
  "ds_read_b128 v[108:111], v139 offset:49152\n\tds_read_b64_tr_b16 v[124:125], v141 offset:9216\n\tds_read_b64_tr_b16 v[126:127], v141 offset:9984\n\t" \
  "s_setprio 0\n\tv_mfma_f32_32x32x16_bf16 v[16:31], v[112:115], v[100:103], v[16:31]\n\ts_setprio 3\n\t" \
  "ds_read_b64_tr_b16 v[128:129], v141 offset:9248\n\tds_read_b64_tr_b16 v[130:131], v141 offset:10016\n\t" \
  "ds_read_b64_tr_b16 v[132:133], v141 offset:9280\n\tds_read_b64_tr_b16 v[134:135], v141 offset:10048\n\ts_setprio 0\n\t" \
  "v_mfma_f32_32x32x16_bf16 v[32:47], v[116:119], v[96:99], v[32:47]\n\ts_setprio 3\n\ts_waitcnt vmcnt(6)\n\tds_add_u32 v153, v151\n\ts_setprio 0\n\t" \
  "v_mfma_f32_32x32x16_bf16 v[48:63], v[116:119], v[100:103], v[48:63]\n\ts_setprio 3\n\ts_add_u32 s44, s44, 196608\n\ts_addc_u32 s45, s45, 0\n\t" \
  "s_setprio 0\n\tv_mfma_f32_32x32x16_bf16 v[64:79], v[120:123], v[96:99], v[64:79]\n\ts_setprio 3\n\ts_add_u32 %[tgt], %[tgt], 4\n\ts_setprio 0\n\t" \
  "v_mfma_f32_32x32x16_bf16 v[80:95], v[120:123], v[100:103], v[80:95]\n\ts_setprio 3\n\tds_read_b32 v152, v150\n\ts_waitcnt lgkmcnt(0)\n\t" \
  "v_readfirstlane_b32 %[t0], v152\n\ts_cmp_lt_u32 %[t0], %[tgt]\n\ts_cbranch_scc1 .Lw1u%=\n\t.Lx1u%=:\n\ts_setprio 0\n\t" \
  "v_mfma_f32_32x32x16_bf16 v[0:15], v[124:127], v[104:107], v[0:15]\n\ts_setprio 3\n\tds_read_b128 v[96:99], v136 offset:0\n\t" \
  "ds_read_b128 v[100:103], v136 offset:4096\n\ts_setprio 0\n\tv_mfma_f32_32x32x16_bf16 v[16:31], v[124:127], v[108:111], v[16:31]\n\ts_setprio 3\n\t" \
  "ds_read_b64_tr_b16 v[112:113], v142 offset:0\n\tds_read_b64_tr_b16 v[114:115], v142 offset:768\n\ts_setprio 0\n\t" \
  "v_mfma_f32_32x32x16_bf16 v[32:47], v[128:131], v[104:107], v[32:47]\n\ts_setprio 3\n\tds_read_b64_tr_b16 v[116:117], v142 offset:32\n\t" \
  "ds_read_b64_tr_b16 v[118:119], v142 offset:800\n\ts_setprio 0\n\tv_mfma_f32_32x32x16_bf16 v[48:63], v[128:131], v[108:111], v[48:63]\n\t" \
  "s_setprio 3\n\tds_read_b64_tr_b16 v[120:121], v142 offset:64\n\tds_read_b64_tr_b16 v[122:123], v142 offset:832\n\ts_add_u32 m0, %[xd], 45056\n\t" \
  "s_setprio 0\n\tv_mfma_f32_32x32x16_bf16 v[64:79], v[132:135], v[104:107], v[64:79]\n\ts_setprio 3\n\tglobal_load_lds_dwordx4 v144, s[46:47]\n\t" \
  "global_load_lds_dwordx4 v145, s[46:47] offset:1024\n\ts_setprio 0\n\tv_mfma_f32_32x32x16_bf16 v[80:95], v[132:135], v[108:111], v[80:95]\n\t" \
  "s_setprio 3\n\tglobal_load_lds_dwordx4 v146, s[46:47] offset:2048\n\tglobal_load_lds_dwordx4 v147, s[46:47] offset:3072\n\ts_add_u32 s46, s46, 128\n\t" \
  "s_addc_u32 s47, s47, 0\n\tv_mov_b32 v152, v140\n\tv_mov_b32 v140, v142\n\tv_mov_b32 v142, v141\n\tv_mov_b32 v141, v152\n\ts_mov_b32 %[t0], %[w0]\n\t" \
  "s_mov_b32 %[w0], %[w2]\n\ts_mov_b32 %[w2], %[w1]\n\ts_mov_b32 %[w1], %[t0]\n\ts_cmp_lg_u32 %[tgt], 40\n\ts_cbranch_scc1 .Lul%=\n\t" \
  "s_waitcnt lgkmcnt(0)\n\ts_setprio 0\n\tv_mfma_f32_32x32x16_bf16 v[0:15], v[112:115], v[96:99], v[0:15]\n\ts_setprio 3\n\t" \
  "ds_read_b128 v[104:107], v137 offset:0\n\tds_read_b128 v[108:111], v137 offset:4096\n\ts_setprio 0\n\t" \
  "v_mfma_f32_32x32x16_bf16 v[16:31], v[112:115], v[100:103], v[16:31]\n\ts_setprio 3\n\tds_read_b64_tr_b16 v[124:125], v140 offset:3072\n\t" \
  "ds_read_b64_tr_b16 v[126:127], v140 offset:3840\n\ts_setprio 0\n\tv_mfma_f32_32x32x16_bf16 v[32:47], v[116:119], v[96:99], v[32:47]\n\ts_setprio 3\n\t" \
  "ds_read_b64_tr_b16 v[128:129], v140 offset:3104\n\tds_read_b64_tr_b16 v[130:131], v140 offset:3872\n\ts_setprio 0\n\t" \
  "v_mfma_f32_32x32x16_bf16 v[48:63], v[116:119], v[100:103], v[48:63]\n\ts_setprio 3\n\tds_read_b64_tr_b16 v[132:133], v140 offset:3136\n\t" \
  "ds_read_b64_tr_b16 v[134:135], v140 offset:3904\n\ts_setprio 0\n\tv_mfma_f32_32x32x16_bf16 v[64:79], v[120:123], v[96:99], v[64:79]\n\ts_setprio 3\n\t" \
  "s_setprio 0\n\tv_mfma_f32_32x32x16_bf16 v[80:95], v[120:123], v[100:103], v[80:95]\n\ts_setprio 3\n\ts_waitcnt lgkmcnt(0)\n\ts_setprio 0\n\t" \
  "v_mfma_f32_32x32x16_bf16 v[0:15], v[124:127], v[104:107], v[0:15]\n\ts_setprio 3\n\tds_read_b128 v[96:99], v138 offset:0\n\t" \
  "ds_read_b128 v[100:103], v138 offset:4096\n\ts_setprio 0\n\tv_mfma_f32_32x32x16_bf16 v[16:31], v[124:127], v[108:111], v[16:31]\n\ts_setprio 3\n\t" \
  "ds_read_b64_tr_b16 v[112:113], v140 offset:6144\n\tds_read_b64_tr_b16 v[114:115], v140 offset:6912\n\ts_setprio 0\n\t" \
  "v_mfma_f32_32x32x16_bf16 v[32:47], v[128:131], v[104:107], v[32:47]\n\ts_setprio 3\n\tds_read_b64_tr_b16 v[116:117], v140 offset:6176\n\t" \
  "ds_read_b64_tr_b16 v[118:119], v140 offset:6944\n\ts_setprio 0\n\tv_mfma_f32_32x32x16_bf16 v[48:63], v[128:131], v[108:111], v[48:63]\n\t" \
  "s_setprio 3\n\tds_read_b64_tr_b16 v[120:121], v140 offset:6208\n\tds_read_b64_tr_b16 v[122:123], v140 offset:6976\n\ts_setprio 0\n\t" \
  "v_mfma_f32_32x32x16_bf16 v[64:79], v[132:135], v[104:107], v[64:79]\n\ts_setprio 3\n\ts_setprio 0\n\t" \
  "v_mfma_f32_32x32x16_bf16 v[80:95], v[132:135], v[108:111], v[80:95]\n\ts_setprio 3\n\ts_waitcnt lgkmcnt(0)\n\ts_setprio 0\n\t" \
  "v_mfma_f32_32x32x16_bf16 v[0:15], v[112:115], v[96:99], v[0:15]\n\ts_setprio 3\n\tds_read_b128 v[104:107], v139 offset:0\n\t" \
  "ds_read_b128 v[108:111], v139 offset:4096\n\tds_read_b64_tr_b16 v[124:125], v140 offset:9216\n\tds_read_b64_tr_b16 v[126:127], v140 offset:9984\n\t" \
  "s_setprio 0\n\tv_mfma_f32_32x32x16_bf16 v[16:31], v[112:115], v[100:103], v[16:31]\n\ts_setprio 3\n\t" \
  "ds_read_b64_tr_b16 v[128:129], v140 offset:9248\n\tds_read_b64_tr_b16 v[130:131], v140 offset:10016\n\t" \
  "ds_read_b64_tr_b16 v[132:133], v140 offset:9280\n\tds_read_b64_tr_b16 v[134:135], v140 offset:10048\n\ts_setprio 0\n\t" \
  "v_mfma_f32_32x32x16_bf16 v[32:47], v[116:119], v[96:99], v[32:47]\n\ts_setprio 3\n\ts_waitcnt vmcnt(0)\n\tds_add_u32 v153, v151\n\ts_setprio 0\n\t" \
  "v_mfma_f32_32x32x16_bf16 v[48:63], v[116:119], v[100:103], v[48:63]\n\ts_setprio 3\n\tglobal_load_dword v154, v158, %[tf] sc1\n\ts_setprio 0\n\t" \
  "v_mfma_f32_32x32x16_bf16 v[64:79], v[120:123], v[96:99], v[64:79]\n\ts_setprio 3\n\ts_add_u32 %[tgt], %[tgt], 4\n\ts_setprio 0\n\t" \
  "v_mfma_f32_32x32x16_bf16 v[80:95], v[120:123], v[100:103], v[80:95]\n\ts_setprio 3\n\tds_read_b32 v152, v150\n\ts_waitcnt lgkmcnt(0)\n\t" \
  "v_readfirstlane_b32 %[t0], v152\n\ts_cmp_lt_u32 %[t0], %[tgt]\n\ts_cbranch_scc1 .Lw2u%=\n\t.Lx2u%=:\n\ts_setprio 0\n\t" \
  "v_mfma_f32_32x32x16_bf16 v[0:15], v[124:127], v[104:107], v[0:15]\n\ts_setprio 3\n\tds_read_b128 v[96:99], v136 offset:45056\n\t" \
  "ds_read_b128 v[100:103], v136 offset:49152\n\ts_setprio 0\n\tv_mfma_f32_32x32x16_bf16 v[16:31], v[124:127], v[108:111], v[16:31]\n\ts_setprio 3\n\t" \
  "ds_read_b64_tr_b16 v[112:113], v141 offset:0\n\tds_read_b64_tr_b16 v[114:115], v141 offset:768\n\ts_setprio 0\n\t" \
  "v_mfma_f32_32x32x16_bf16 v[32:47], v[128:131], v[104:107], v[32:47]\n\ts_setprio 3\n\tds_read_b64_tr_b16 v[116:117], v141 offset:32\n\t" \
  "ds_read_b64_tr_b16 v[118:119], v141 offset:800\n\ts_setprio 0\n\tv_mfma_f32_32x32x16_bf16 v[48:63], v[128:131], v[108:111], v[48:63]\n\t" \
  "s_setprio 3\n\tds_read_b64_tr_b16 v[120:121], v141 offset:64\n\tds_read_b64_tr_b16 v[122:123], v141 offset:832\n\ts_setprio 0\n\t" \
  "v_mfma_f32_32x32x16_bf16 v[64:79], v[132:135], v[104:107], v[64:79]\n\ts_setprio 3\n\ts_setprio 0\n\t" \
  "v_mfma_f32_32x32x16_bf16 v[80:95], v[132:135], v[108:111], v[80:95]\n\ts_setprio 3\n\ts_waitcnt lgkmcnt(0)\n\ts_setprio 0\n\t" \
  "v_mfma_f32_32x32x16_bf16 v[0:15], v[112:115], v[96:99], v[0:15]\n\ts_setprio 3\n\tds_read_b128 v[104:107], v137 offset:45056\n\t" \
  "ds_read_b128 v[108:111], v137 offset:49152\n\ts_setprio 0\n\tv_mfma_f32_32x32x16_bf16 v[16:31], v[112:115], v[100:103], v[16:31]\n\ts_setprio 3\n\t" \
  "ds_read_b64_tr_b16 v[124:125], v141 offset:3072\n\tds_read_b64_tr_b16 v[126:127], v141 offset:3840\n\ts_setprio 0\n\t" \
  "v_mfma_f32_32x32x16_bf16 v[32:47], v[116:119], v[96:99], v[32:47]\n\ts_setprio 3\n\tds_read_b64_tr_b16 v[128:129], v141 offset:3104\n\t" \
  "ds_read_b64_tr_b16 v[130:131], v141 offset:3872\n\ts_setprio 0\n\tv_mfma_f32_32x32x16_bf16 v[48:63], v[116:119], v[100:103], v[48:63]\n\t" \
  "s_setprio 3\n\tds_read_b64_tr_b16 v[132:133], v141 offset:3136\n\tds_read_b64_tr_b16 v[134:135], v141 offset:3904\n\ts_setprio 0\n\t" \
  "v_mfma_f32_32x32x16_bf16 v[64:79], v[120:123], v[96:99], v[64:79]\n\ts_setprio 3\n\ts_setprio 0\n\t" \
  "v_mfma_f32_32x32x16_bf16 v[80:95], v[120:123], v[100:103], v[80:95]\n\ts_setprio 3\n\ts_waitcnt lgkmcnt(0)\n\ts_setprio 0\n\t" \
  "v_mfma_f32_32x32x16_bf16 v[0:15], v[124:127], v[104:107], v[0:15]\n\ts_setprio 3\n\tds_read_b128 v[96:99], v138 offset:45056\n\t" \
  "ds_read_b128 v[100:103], v138 offset:49152\n\ts_setprio 0\n\tv_mfma_f32_32x32x16_bf16 v[16:31], v[124:127], v[108:111], v[16:31]\n\ts_setprio 3\n\t" \
  "ds_read_b64_tr_b16 v[112:113], v141 offset:6144\n\tds_read_b64_tr_b16 v[114:115], v141 offset:6912\n\ts_setprio 0\n\t" \
  "v_mfma_f32_32x32x16_bf16 v[32:47], v[128:131], v[104:107], v[32:47]\n\ts_setprio 3\n\tds_read_b64_tr_b16 v[116:117], v141 offset:6176\n\t" \
  "ds_read_b64_tr_b16 v[118:119], v141 offset:6944\n\ts_setprio 0\n\tv_mfma_f32_32x32x16_bf16 v[48:63], v[128:131], v[108:111], v[48:63]\n\t" \
  "s_setprio 3\n\tds_read_b64_tr_b16 v[120:121], v141 offset:6208\n\tds_read_b64_tr_b16 v[122:123], v141 offset:6976\n\ts_setprio 0\n\t" \
  "v_mfma_f32_32x32x16_bf16 v[64:79], v[132:135], v[104:107], v[64:79]\n\ts_setprio 3\n\ts_setprio 0\n\t" \
  "v_mfma_f32_32x32x16_bf16 v[80:95], v[132:135], v[108:111], v[80:95]\n\ts_setprio 3\n\ts_waitcnt lgkmcnt(0)\n\ts_setprio 0\n\t" \
  "v_mfma_f32_32x32x16_bf16 v[0:15], v[112:115], v[96:99], v[0:15]\n\ts_setprio 3\n\tds_read_b128 v[104:107], v139 offset:45056\n\t" \
  "ds_read_b128 v[108:111], v139 offset:49152\n\ts_setprio 0\n\tv_mfma_f32_32x32x16_bf16 v[16:31], v[112:115], v[100:103], v[16:31]\n\ts_setprio 3\n\t" \
  "ds_read_b64_tr_b16 v[124:125], v141 offset:9216\n\tds_read_b64_tr_b16 v[126:127], v141 offset:9984\n\ts_setprio 0\n\t" \
  "v_mfma_f32_32x32x16_bf16 v[32:47], v[116:119], v[96:99], v[32:47]\n\ts_setprio 3\n\tds_read_b64_tr_b16 v[128:129], v141 offset:9248\n\t" \
  "ds_read_b64_tr_b16 v[130:131], v141 offset:10016\n\ts_setprio 0\n\tv_mfma_f32_32x32x16_bf16 v[48:63], v[116:119], v[100:103], v[48:63]\n\t" \
  "s_setprio 3\n\tds_read_b64_tr_b16 v[132:133], v141 offset:9280\n\tds_read_b64_tr_b16 v[134:135], v141 offset:10048\n\ts_setprio 0\n\t" \
  "v_mfma_f32_32x32x16_bf16 v[64:79], v[120:123], v[96:99], v[64:79]\n\ts_setprio 3\n\tds_add_u32 v153, v151\n\ts_setprio 0\n\t" \
  "v_mfma_f32_32x32x16_bf16 v[80:95], v[120:123], v[100:103], v[80:95]\n\ts_setprio 3\n\ts_waitcnt lgkmcnt(0)\n\ts_setprio 0\n\t" \
  "v_mfma_f32_32x32x16_bf16 v[0:15], v[124:127], v[104:107], v[0:15]\n\ts_setprio 3\n\ts_setprio 0\n\t" \
  "v_mfma_f32_32x32x16_bf16 v[16:31], v[124:127], v[108:111], v[16:31]\n\ts_setprio 3\n\ts_setprio 0\n\t" \
  "v_mfma_f32_32x32x16_bf16 v[32:47], v[128:131], v[104:107], v[32:47]\n\ts_setprio 3\n\ts_setprio 0\n\t" \
  "v_mfma_f32_32x32x16_bf16 v[48:63], v[128:131], v[108:111], v[48:63]\n\ts_setprio 3\n\ts_setprio 0\n\t" \
  "v_mfma_f32_32x32x16_bf16 v[64:79], v[132:135], v[104:107], v[64:79]\n\ts_setprio 3\n\ts_setprio 0\n\t" \
  "v_mfma_f32_32x32x16_bf16 v[80:95], v[132:135], v[108:111], v[80:95]\n\ts_setprio 3\n\ts_waitcnt vmcnt(0)\n\ts_branch .Lhu%=\n\t.Lw0u%=:\n\t" \
  "s_mov_b32 %[t1], 0x100000\n\t.Ly0u%=:\n\tds_read_b32 v152, v150\n\ts_waitcnt lgkmcnt(0)\n\tv_readfirstlane_b32 %[t0], v152\n\t" \
  "s_cmp_ge_u32 %[t0], %[tgt]\n\ts_cbranch_scc1 .Lx0u%=\n\ts_sleep 1\n\ts_sub_u32 %[t1], %[t1], 1\n\ts_cbranch_scc0 .Ly0u%=\n\ts_branch .Lx0u%=\n\t" \
  ".Lw1u%=:\n\ts_mov_b32 %[t1], 0x100000\n\t.Ly1u%=:\n\tds_read_b32 v152, v150\n\ts_waitcnt lgkmcnt(0)\n\tv_readfirstlane_b32 %[t0], v152\n\t" \
  "s_cmp_ge_u32 %[t0], %[tgt]\n\ts_cbranch_scc1 .Lx1u%=\n\ts_sleep 1\n\ts_sub_u32 %[t1], %[t1], 1\n\ts_cbranch_scc0 .Ly1u%=\n\ts_branch .Lx1u%=\n\t" \
  ".Lw2u%=:\n\ts_mov_b32 %[t1], 0x100000\n\t.Ly2u%=:\n\tds_read_b32 v152, v150\n\ts_waitcnt lgkmcnt(0)\n\tv_readfirstlane_b32 %[t0], v152\n\t" \
  "s_cmp_ge_u32 %[t0], %[tgt]\n\ts_cbranch_scc1 .Lx2u%=\n\ts_sleep 1\n\ts_sub_u32 %[t1], %[t1], 1\n\ts_cbranch_scc0 .Ly2u%=\n\ts_branch .Lx2u%=\n\t" \
  ".Lhu%=:\n\ts_nop 15\n\t"
#define UP_CLOB "v96","v97","v98","v99","v100","v101","v102","v103","v104","v105","v106","v107","v108","v109","v110","v111","v112","v113","v114","v115","v116","v117","v118","v119","v120","v121","v122","v123","v124","v125","v126","v127","v128","v129","v130","v131","v132","v133","v134","v135","v152"
#define DN_LOOP \
  "global_load_dwordx4 v[100:103], v148, s[44:45] sc1\n\tglobal_load_dwordx4 v[104:107], v149, s[44:45] sc1\n\t" \
  "global_load_dwordx4 v[108:111], v150, s[44:45] sc1\n\tglobal_load_dwordx4 v[112:115], v151, s[44:45] sc1\n\ts_add_u32 s44, s44, 1024\n\t" \
  "s_addc_u32 s45, s45, 0\n\tglobal_load_dwordx4 v[116:119], v148, s[44:45] sc1\n\tglobal_load_dwordx4 v[120:123], v149, s[44:45] sc1\n\t" \
  "global_load_dwordx4 v[124:127], v150, s[44:45] sc1\n\tglobal_load_dwordx4 v[128:131], v151, s[44:45] sc1\n\ts_add_u32 s44, s44, 1024\n\t" \
  "s_addc_u32 s45, s45, 0\n\tglobal_load_dwordx4 v[132:135], v148, s[44:45] sc1\n\tglobal_load_dwordx4 v[136:139], v149, s[44:45] sc1\n\t" \
  "global_load_dwordx4 v[140:143], v150, s[44:45] sc1\n\tglobal_load_dwordx4 v[144:147], v151, s[44:45] sc1\n\ts_add_u32 s44, s44, 1024\n\t" \
  "s_addc_u32 s45, s45, 0\n\tv_mov_b32 v152, v160\n\ts_mov_b32 %[ro], 0\n\tds_read_b64_tr_b16 v[80:81], v160 offset:0\n\t" \
  "ds_read_b64_tr_b16 v[82:83], v160 offset:1280\n\tds_read_b64_tr_b16 v[84:85], v160 offset:32\n\tds_read_b64_tr_b16 v[86:87], v160 offset:1312\n\t" \
  "ds_read_b64_tr_b16 v[88:89], v160 offset:64\n\tds_read_b64_tr_b16 v[90:91], v160 offset:1344\n\tds_read_b64_tr_b16 v[92:93], v160 offset:96\n\t" \
  "ds_read_b64_tr_b16 v[94:95], v160 offset:1376\n\tds_read_b64_tr_b16 v[96:97], v160 offset:128\n\tds_read_b64_tr_b16 v[98:99], v160 offset:1408\n\t" \
  "s_waitcnt vmcnt(8)\n\ts_waitcnt lgkmcnt(8)\n\ts_setprio 0\n\tv_mfma_f32_16x16x32_bf16 v[0:3], v[80:83], v[100:103], 0\n\ts_waitcnt lgkmcnt(6)\n\t" \
  "v_mfma_f32_16x16x32_bf16 v[16:19], v[84:87], v[100:103], 0\n\tv_mfma_f32_16x16x32_bf16 v[4:7], v[80:83], v[104:107], 0\n\ts_waitcnt lgkmcnt(4)\n\t" \
  "v_mfma_f32_16x16x32_bf16 v[32:35], v[88:91], v[100:103], 0\n\tv_mfma_f32_16x16x32_bf16 v[20:23], v[84:87], v[104:107], 0\n\t" \
  "v_mfma_f32_16x16x32_bf16 v[8:11], v[80:83], v[108:111], 0\n\ts_waitcnt lgkmcnt(2)\n\tv_mfma_f32_16x16x32_bf16 v[48:51], v[92:95], v[100:103], 0\n\t" \
  "v_mfma_f32_16x16x32_bf16 v[36:39], v[88:91], v[104:107], 0\n\ts_setprio 3\n\ts_add_u32 %[t1], %[ro], 5120\n\ts_cmp_eq_u32 %[t1], 25600\n\t" \
  "s_cselect_b32 %[t1], 0, %[t1]\n\tv_add_u32 v160, %[t1], v152\n\ts_setprio 0\n\tv_mfma_f32_16x16x32_bf16 v[24:27], v[84:87], v[108:111], 0\n\t" \
  "v_mfma_f32_16x16x32_bf16 v[12:15], v[80:83], v[112:115], 0\n\ts_setprio 3\n\tds_read_b64_tr_b16 v[80:81], v160 offset:0\n\t" \
  "ds_read_b64_tr_b16 v[82:83], v160 offset:1280\n\ts_waitcnt lgkmcnt(2)\n\ts_setprio 0\n\tv_mfma_f32_16x16x32_bf16 v[64:67], v[96:99], v[100:103], 0\n\t" \
  "s_setprio 3\n\tglobal_load_dwordx4 v[100:103], v148, s[44:45] sc1\n\ts_setprio 0\n\tv_mfma_f32_16x16x32_bf16 v[52:55], v[92:95], v[104:107], 0\n\t" \
  "s_setprio 3\n\ts_add_u32 m0, %[ring], %[ro]\n\ts_mov_b32 %[ro], %[t1]\n\tglobal_load_lds_dwordx4 v162, s[42:43] sc1\n\t" \
  "global_load_lds_dwordx4 v162, s[42:43] offset:1024 sc1\n\ts_setprio 0\n\tv_mfma_f32_16x16x32_bf16 v[40:43], v[88:91], v[108:111], 0\n\ts_setprio 3\n\t" \
  "global_load_lds_dwordx4 v162, s[42:43] offset:2048 sc1\n\tglobal_load_lds_dwordx4 v162, s[42:43] offset:3072 sc1\n\ts_setprio 0\n\t" \
  "v_mfma_f32_16x16x32_bf16 v[28:31], v[84:87], v[112:115], 0\n\ts_setprio 3\n\tds_read_b64_tr_b16 v[84:85], v160 offset:32\n\t" \
  "ds_read_b64_tr_b16 v[86:87], v160 offset:1312\n\ts_setprio 0\n\tv_mfma_f32_16x16x32_bf16 v[68:71], v[96:99], v[104:107], 0\n\ts_setprio 3\n\t" \
  "global_load_dwordx4 v[104:107], v149, s[44:45] sc1\n\ts_setprio 0\n\tv_mfma_f32_16x16x32_bf16 v[56:59], v[92:95], v[108:111], 0\n\ts_setprio 3\n\t" \
  "s_add_u32 m0, m0, 1024\n\ts_add_u32 s42, s42, 1024\n\ts_addc_u32 s43, s43, 0\n\tglobal_load_lds_dwordx4 v162, s[42:43] offset:3072 sc1\n\t" \
  "s_setprio 0\n\tv_mfma_f32_16x16x32_bf16 v[44:47], v[88:91], v[112:115], 0\n\ts_setprio 3\n\tds_read_b64_tr_b16 v[88:89], v160 offset:64\n\t" \
  "ds_read_b64_tr_b16 v[90:91], v160 offset:1344\n\ts_add_u32 s42, s42, 4096\n\ts_addc_u32 s43, s43, 0\n\ts_setprio 0\n\t" \
  "v_mfma_f32_16x16x32_bf16 v[72:75], v[96:99], v[108:111], 0\n\ts_setprio 3\n\tglobal_load_dwordx4 v[108:111], v150, s[44:45] sc1\n\ts_setprio 0\n\t" \
  "v_mfma_f32_16x16x32_bf16 v[60:63], v[92:95], v[112:115], 0\n\ts_setprio 3\n\tds_read_b64_tr_b16 v[92:93], v160 offset:96\n\t" \
  "ds_read_b64_tr_b16 v[94:95], v160 offset:1376\n\ts_setprio 0\n\tv_mfma_f32_16x16x32_bf16 v[76:79], v[96:99], v[112:115], 0\n\ts_setprio 3\n\t" \
  "ds_read_b64_tr_b16 v[96:97], v160 offset:128\n\tds_read_b64_tr_b16 v[98:99], v160 offset:1408\n\t" \
  "global_load_dwordx4 v[112:115], v151, s[44:45] sc1\n\ts_add_u32 s44, s44, 1024\n\ts_addc_u32 s45, s45, 0\n\ts_waitcnt vmcnt(13)\n\t" \
  "s_waitcnt lgkmcnt(8)\n\ts_setprio 0\n\tv_mfma_f32_16x16x32_bf16 v[0:3], v[80:83], v[116:119], v[0:3]\n\ts_waitcnt lgkmcnt(6)\n\t" \
  "v_mfma_f32_16x16x32_bf16 v[16:19], v[84:87], v[116:119], v[16:19]\n\tv_mfma_f32_16x16x32_bf16 v[4:7], v[80:83], v[120:123], v[4:7]\n\t" \
  "s_waitcnt lgkmcnt(4)\n\tv_mfma_f32_16x16x32_bf16 v[32:35], v[88:91], v[116:119], v[32:35]\n\t" \
  "v_mfma_f32_16x16x32_bf16 v[20:23], v[84:87], v[120:123], v[20:23]\n\tv_mfma_f32_16x16x32_bf16 v[8:11], v[80:83], v[124:127], v[8:11]\n\t" \
  "s_waitcnt lgkmcnt(2)\n\tv_mfma_f32_16x16x32_bf16 v[48:51], v[92:95], v[116:119], v[48:51]\n\t" \
  "v_mfma_f32_16x16x32_bf16 v[36:39], v[88:91], v[120:123], v[36:39]\n\ts_setprio 3\n\ts_add_u32 %[t1], %[ro], 5120\n\ts_cmp_eq_u32 %[t1], 25600\n\t" \
  "s_cselect_b32 %[t1], 0, %[t1]\n\tv_add_u32 v160, %[t1], v152\n\ts_setprio 0\n\tv_mfma_f32_16x16x32_bf16 v[24:27], v[84:87], v[124:127], v[24:27]\n\t" \
  "v_mfma_f32_16x16x32_bf16 v[12:15], v[80:83], v[128:131], v[12:15]\n\ts_setprio 3\n\tds_read_b64_tr_b16 v[80:81], v160 offset:0\n\t" \
  "ds_read_b64_tr_b16 v[82:83], v160 offset:1280\n\ts_waitcnt lgkmcnt(2)\n\ts_setprio 0\n\t" \
  "v_mfma_f32_16x16x32_bf16 v[64:67], v[96:99], v[116:119], v[64:67]\n\ts_setprio 3\n\tglobal_load_dwordx4 v[116:119], v148, s[44:45] sc1\n\t" \
  "s_setprio 0\n\tv_mfma_f32_16x16x32_bf16 v[52:55], v[92:95], v[120:123], v[52:55]\n\ts_setprio 3\n\ts_add_u32 m0, %[ring], %[ro]\n\t" \
  "s_mov_b32 %[ro], %[t1]\n\tglobal_load_lds_dwordx4 v162, s[42:43] sc1\n\tglobal_load_lds_dwordx4 v162, s[42:43] offset:1024 sc1\n\ts_setprio 0\n\t" \
  "v_mfma_f32_16x16x32_bf16 v[40:43], v[88:91], v[124:127], v[40:43]\n\ts_setprio 3\n\tglobal_load_lds_dwordx4 v162, s[42:43] offset:2048 sc1\n\t" \
  "global_load_lds_dwordx4 v162, s[42:43] offset:3072 sc1\n\ts_setprio 0\n\tv_mfma_f32_16x16x32_bf16 v[28:31], v[84:87], v[128:131], v[28:31]\n\t" \
  "s_setprio 3\n\tds_read_b64_tr_b16 v[84:85], v160 offset:32\n\tds_read_b64_tr_b16 v[86:87], v160 offset:1312\n\ts_setprio 0\n\t" \
  "v_mfma_f32_16x16x32_bf16 v[68:71], v[96:99], v[120:123], v[68:71]\n\ts_setprio 3\n\tglobal_load_dwordx4 v[120:123], v149, s[44:45] sc1\n\t" \
  "s_setprio 0\n\tv_mfma_f32_16x16x32_bf16 v[56:59], v[92:95], v[124:127], v[56:59]\n\ts_setprio 3\n\ts_add_u32 m0, m0, 1024\n\t" \
  "s_add_u32 s42, s42, 1024\n\ts_addc_u32 s43, s43, 0\n\tglobal_load_lds_dwordx4 v162, s[42:43] offset:3072 sc1\n\ts_setprio 0\n\t" \
  "v_mfma_f32_16x16x32_bf16 v[44:47], v[88:91], v[128:131], v[44:47]\n\ts_setprio 3\n\tds_read_b64_tr_b16 v[88:89], v160 offset:64\n\t" \
  "ds_read_b64_tr_b16 v[90:91], v160 offset:1344\n\ts_add_u32 s42, s42, 4096\n\ts_addc_u32 s43, s43, 0\n\ts_setprio 0\n\t" \
  "v_mfma_f32_16x16x32_bf16 v[72:75], v[96:99], v[124:127], v[72:75]\n\ts_setprio 3\n\tglobal_load_dwordx4 v[124:127], v150, s[44:45] sc1\n\t" \
  "s_setprio 0\n\tv_mfma_f32_16x16x32_bf16 v[60:63], v[92:95], v[128:131], v[60:63]\n\ts_setprio 3\n\tds_read_b64_tr_b16 v[92:93], v160 offset:96\n\t" \
  "ds_read_b64_tr_b16 v[94:95], v160 offset:1376\n\ts_setprio 0\n\tv_mfma_f32_16x16x32_bf16 v[76:79], v[96:99], v[128:131], v[76:79]\n\ts_setprio 3\n\t" \
  "ds_read_b64_tr_b16 v[96:97], v160 offset:128\n\tds_read_b64_tr_b16 v[98:99], v160 offset:1408\n\t" \
  "global_load_dwordx4 v[128:131], v151, s[44:45] sc1\n\ts_add_u32 s44, s44, 1024\n\ts_addc_u32 s45, s45, 0\n\ts_waitcnt vmcnt(18)\n\t" \
  "s_waitcnt lgkmcnt(8)\n\ts_setprio 0\n\tv_mfma_f32_16x16x32_bf16 v[0:3], v[80:83], v[132:135], v[0:3]\n\ts_waitcnt lgkmcnt(6)\n\t" \
  "v_mfma_f32_16x16x32_bf16 v[16:19], v[84:87], v[132:135], v[16:19]\n\tv_mfma_f32_16x16x32_bf16 v[4:7], v[80:83], v[136:139], v[4:7]\n\t" \
  "s_waitcnt lgkmcnt(4)\n\tv_mfma_f32_16x16x32_bf16 v[32:35], v[88:91], v[132:135], v[32:35]\n\t" \
  "v_mfma_f32_16x16x32_bf16 v[20:23], v[84:87], v[136:139], v[20:23]\n\tv_mfma_f32_16x16x32_bf16 v[8:11], v[80:83], v[140:143], v[8:11]\n\t" \
  "s_waitcnt lgkmcnt(2)\n\tv_mfma_f32_16x16x32_bf16 v[48:51], v[92:95], v[132:135], v[48:51]\n\t" \
  "v_mfma_f32_16x16x32_bf16 v[36:39], v[88:91], v[136:139], v[36:39]\n\ts_setprio 3\n\ts_add_u32 %[t1], %[ro], 5120\n\ts_cmp_eq_u32 %[t1], 25600\n\t" \
  "s_cselect_b32 %[t1], 0, %[t1]\n\tv_add_u32 v160, %[t1], v152\n\ts_setprio 0\n\tv_mfma_f32_16x16x32_bf16 v[24:27], v[84:87], v[140:143], v[24:27]\n\t" \
  "v_mfma_f32_16x16x32_bf16 v[12:15], v[80:83], v[144:147], v[12:15]\n\ts_setprio 3\n\tds_read_b64_tr_b16 v[80:81], v160 offset:0\n\t" \
  "ds_read_b64_tr_b16 v[82:83], v160 offset:1280\n\ts_waitcnt lgkmcnt(2)\n\ts_setprio 0\n\t" \
  "v_mfma_f32_16x16x32_bf16 v[64:67], v[96:99], v[132:135], v[64:67]\n\ts_setprio 3\n\tglobal_load_dwordx4 v[132:135], v148, s[44:45] sc1\n\t" \
  "s_setprio 0\n\tv_mfma_f32_16x16x32_bf16 v[52:55], v[92:95], v[136:139], v[52:55]\n\ts_setprio 3\n\ts_add_u32 m0, %[ring], %[ro]\n\t" \
  "s_mov_b32 %[ro], %[t1]\n\tglobal_load_lds_dwordx4 v162, s[42:43] sc1\n\tglobal_load_lds_dwordx4 v162, s[42:43] offset:1024 sc1\n\ts_setprio 0\n\t" \
  "v_mfma_f32_16x16x32_bf16 v[40:43], v[88:91], v[140:143], v[40:43]\n\ts_setprio 3\n\tglobal_load_lds_dwordx4 v162, s[42:43] offset:2048 sc1\n\t" \
  "global_load_lds_dwordx4 v162, s[42:43] offset:3072 sc1\n\ts_setprio 0\n\tv_mfma_f32_16x16x32_bf16 v[28:31], v[84:87], v[144:147], v[28:31]\n\t" \
  "s_setprio 3\n\tds_read_b64_tr_b16 v[84:85], v160 offset:32\n\tds_read_b64_tr_b16 v[86:87], v160 offset:1312\n\ts_setprio 0\n\t" \
  "v_mfma_f32_16x16x32_bf16 v[68:71], v[96:99], v[136:139], v[68:71]\n\ts_setprio 3\n\tglobal_load_dwordx4 v[136:139], v149, s[44:45] sc1\n\t" \
  "s_setprio 0\n\tv_mfma_f32_16x16x32_bf16 v[56:59], v[92:95], v[140:143], v[56:59]\n\ts_setprio 3\n\ts_add_u32 m0, m0, 1024\n\t" \
  "s_add_u32 s42, s42, 1024\n\ts_addc_u32 s43, s43, 0\n\tglobal_load_lds_dwordx4 v162, s[42:43] offset:3072 sc1\n\ts_setprio 0\n\t" \
  "v_mfma_f32_16x16x32_bf16 v[44:47], v[88:91], v[144:147], v[44:47]\n\ts_setprio 3\n\tds_read_b64_tr_b16 v[88:89], v160 offset:64\n\t" \
  "ds_read_b64_tr_b16 v[90:91], v160 offset:1344\n\ts_add_u32 s42, s42, 4096\n\ts_addc_u32 s43, s43, 0\n\ts_setprio 0\n\t" \
  "v_mfma_f32_16x16x32_bf16 v[72:75], v[96:99], v[140:143], v[72:75]\n\ts_setprio 3\n\tglobal_load_dwordx4 v[140:143], v150, s[44:45] sc1\n\t" \
  "s_setprio 0\n\tv_mfma_f32_16x16x32_bf16 v[60:63], v[92:95], v[144:147], v[60:63]\n\ts_setprio 3\n\tds_read_b64_tr_b16 v[92:93], v160 offset:96\n\t" \
  "ds_read_b64_tr_b16 v[94:95], v160 offset:1376\n\ts_setprio 0\n\tv_mfma_f32_16x16x32_bf16 v[76:79], v[96:99], v[144:147], v[76:79]\n\ts_setprio 3\n\t" \
  "ds_read_b64_tr_b16 v[96:97], v160 offset:128\n\tds_read_b64_tr_b16 v[98:99], v160 offset:1408\n\t" \
  "global_load_dwordx4 v[144:147], v151, s[44:45] sc1\n\ts_add_u32 s44, s44, 1024\n\ts_addc_u32 s45, s45, 0\n\ts_mov_b32 %[t0], 3\n\t.Ldl%=:\n\t" \
  "s_cmp_eq_u32 %[t0], 5\n\ts_cbranch_scc1 .Lfa%=\n\t.Lha%=:\n\ts_add_u32 %[t0], %[t0], 1\n\ts_waitcnt vmcnt(18)\n\ts_waitcnt lgkmcnt(8)\n\t" \
  "s_setprio 0\n\tv_mfma_f32_16x16x32_bf16 v[0:3], v[80:83], v[100:103], v[0:3]\n\ts_waitcnt lgkmcnt(6)\n\t" \
  "v_mfma_f32_16x16x32_bf16 v[16:19], v[84:87], v[100:103], v[16:19]\n\tv_mfma_f32_16x16x32_bf16 v[4:7], v[80:83], v[104:107], v[4:7]\n\t" \
  "s_waitcnt lgkmcnt(4)\n\tv_mfma_f32_16x16x32_bf16 v[32:35], v[88:91], v[100:103], v[32:35]\n\t" \
  "v_mfma_f32_16x16x32_bf16 v[20:23], v[84:87], v[104:107], v[20:23]\n\tv_mfma_f32_16x16x32_bf16 v[8:11], v[80:83], v[108:111], v[8:11]\n\t" \
  "s_waitcnt lgkmcnt(2)\n\tv_mfma_f32_16x16x32_bf16 v[48:51], v[92:95], v[100:103], v[48:51]\n\t" \
  "v_mfma_f32_16x16x32_bf16 v[36:39], v[88:91], v[104:107], v[36:39]\n\ts_setprio 3\n\ts_add_u32 %[t1], %[ro], 5120\n\ts_cmp_eq_u32 %[t1], 25600\n\t" \
  "s_cselect_b32 %[t1], 0, %[t1]\n\tv_add_u32 v160, %[t1], v152\n\ts_setprio 0\n\tv_mfma_f32_16x16x32_bf16 v[24:27], v[84:87], v[108:111], v[24:27]\n\t" \
  "v_mfma_f32_16x16x32_bf16 v[12:15], v[80:83], v[112:115], v[12:15]\n\ts_setprio 3\n\tds_read_b64_tr_b16 v[80:81], v160 offset:0\n\t" \
  "ds_read_b64_tr_b16 v[82:83], v160 offset:1280\n\ts_waitcnt lgkmcnt(2)\n\ts_setprio 0\n\t" \
  "v_mfma_f32_16x16x32_bf16 v[64:67], v[96:99], v[100:103], v[64:67]\n\ts_setprio 3\n\tglobal_load_dwordx4 v[100:103], v148, s[44:45] sc1\n\t" \
  "s_setprio 0\n\tv_mfma_f32_16x16x32_bf16 v[52:55], v[92:95], v[104:107], v[52:55]\n\ts_setprio 3\n\ts_add_u32 m0, %[ring], %[ro]\n\t" \
  "s_mov_b32 %[ro], %[t1]\n\tglobal_load_lds_dwordx4 v162, s[42:43] sc1\n\tglobal_load_lds_dwordx4 v162, s[42:43] offset:1024 sc1\n\ts_setprio 0\n\t" \
  "v_mfma_f32_16x16x32_bf16 v[40:43], v[88:91], v[108:111], v[40:43]\n\ts_setprio 3\n\tglobal_load_lds_dwordx4 v162, s[42:43] offset:2048 sc1\n\t" \
  "global_load_lds_dwordx4 v162, s[42:43] offset:3072 sc1\n\ts_setprio 0\n\tv_mfma_f32_16x16x32_bf16 v[28:31], v[84:87], v[112:115], v[28:31]\n\t" \
  "s_setprio 3\n\tds_read_b64_tr_b16 v[84:85], v160 offset:32\n\tds_read_b64_tr_b16 v[86:87], v160 offset:1312\n\ts_setprio 0\n\t" \
  "v_mfma_f32_16x16x32_bf16 v[68:71], v[96:99], v[104:107], v[68:71]\n\ts_setprio 3\n\tglobal_load_dwordx4 v[104:107], v149, s[44:45] sc1\n\t" \
  "s_setprio 0\n\tv_mfma_f32_16x16x32_bf16 v[56:59], v[92:95], v[108:111], v[56:59]\n\ts_setprio 3\n\ts_add_u32 m0, m0, 1024\n\t" \
  "s_add_u32 s42, s42, 1024\n\ts_addc_u32 s43, s43, 0\n\tglobal_load_lds_dwordx4 v162, s[42:43] offset:3072 sc1\n\ts_setprio 0\n\t" \
  "v_mfma_f32_16x16x32_bf16 v[44:47], v[88:91], v[112:115], v[44:47]\n\ts_setprio 3\n\tds_read_b64_tr_b16 v[88:89], v160 offset:64\n\t" \
  "ds_read_b64_tr_b16 v[90:91], v160 offset:1344\n\ts_add_u32 s42, s42, 4096\n\ts_addc_u32 s43, s43, 0\n\ts_setprio 0\n\t" \
  "v_mfma_f32_16x16x32_bf16 v[72:75], v[96:99], v[108:111], v[72:75]\n\ts_setprio 3\n\tglobal_load_dwordx4 v[108:111], v150, s[44:45] sc1\n\t" \
  "s_setprio 0\n\tv_mfma_f32_16x16x32_bf16 v[60:63], v[92:95], v[112:115], v[60:63]\n\ts_setprio 3\n\tds_read_b64_tr_b16 v[92:93], v160 offset:96\n\t" \
  "ds_read_b64_tr_b16 v[94:95], v160 offset:1376\n\ts_setprio 0\n\tv_mfma_f32_16x16x32_bf16 v[76:79], v[96:99], v[112:115], v[76:79]\n\ts_setprio 3\n\t" \
  "ds_read_b64_tr_b16 v[96:97], v160 offset:128\n\tds_read_b64_tr_b16 v[98:99], v160 offset:1408\n\t" \
  "global_load_dwordx4 v[112:115], v151, s[44:45] sc1\n\ts_add_u32 s44, s44, 1024\n\ts_addc_u32 s45, s45, 0\n\ts_cmp_eq_u32 %[t0], 5\n\t" \
  "s_cbranch_scc1 .Lfb%=\n\t.Lhb%=:\n\ts_add_u32 %[t0], %[t0], 1\n\ts_waitcnt vmcnt(18)\n\ts_waitcnt lgkmcnt(8)\n\ts_setprio 0\n\t" \
  "v_mfma_f32_16x16x32_bf16 v[0:3], v[80:83], v[116:119], v[0:3]\n\ts_waitcnt lgkmcnt(6)\n\t" \
  "v_mfma_f32_16x16x32_bf16 v[16:19], v[84:87], v[116:119], v[16:19]\n\tv_mfma_f32_16x16x32_bf16 v[4:7], v[80:83], v[120:123], v[4:7]\n\t" \
  "s_waitcnt lgkmcnt(4)\n\tv_mfma_f32_16x16x32_bf16 v[32:35], v[88:91], v[116:119], v[32:35]\n\t" \
  "v_mfma_f32_16x16x32_bf16 v[20:23], v[84:87], v[120:123], v[20:23]\n\tv_mfma_f32_16x16x32_bf16 v[8:11], v[80:83], v[124:127], v[8:11]\n\t" \
  "s_waitcnt lgkmcnt(2)\n\tv_mfma_f32_16x16x32_bf16 v[48:51], v[92:95], v[116:119], v[48:51]\n\t" \
  "v_mfma_f32_16x16x32_bf16 v[36:39], v[88:91], v[120:123], v[36:39]\n\ts_setprio 3\n\ts_add_u32 %[t1], %[ro], 5120\n\ts_cmp_eq_u32 %[t1], 25600\n\t" \
  "s_cselect_b32 %[t1], 0, %[t1]\n\tv_add_u32 v160, %[t1], v152\n\ts_setprio 0\n\tv_mfma_f32_16x16x32_bf16 v[24:27], v[84:87], v[124:127], v[24:27]\n\t" \
  "v_mfma_f32_16x16x32_bf16 v[12:15], v[80:83], v[128:131], v[12:15]\n\ts_setprio 3\n\tds_read_b64_tr_b16 v[80:81], v160 offset:0\n\t" \
  "ds_read_b64_tr_b16 v[82:83], v160 offset:1280\n\ts_waitcnt lgkmcnt(2)\n\ts_setprio 0\n\t" \
  "v_mfma_f32_16x16x32_bf16 v[64:67], v[96:99], v[116:119], v[64:67]\n\ts_setprio 3\n\tglobal_load_dwordx4 v[116:119], v148, s[44:45] sc1\n\t" \
  "s_setprio 0\n\tv_mfma_f32_16x16x32_bf16 v[52:55], v[92:95], v[120:123], v[52:55]\n\ts_setprio 3\n\ts_add_u32 m0, %[ring], %[ro]\n\t" \
  "s_mov_b32 %[ro], %[t1]\n\tglobal_load_lds_dwordx4 v162, s[42:43] sc1\n\tglobal_load_lds_dwordx4 v162, s[42:43] offset:1024 sc1\n\ts_setprio 0\n\t" \
  "v_mfma_f32_16x16x32_bf16 v[40:43], v[88:91], v[124:127], v[40:43]\n\ts_setprio 3\n\tglobal_load_lds_dwordx4 v162, s[42:43] offset:2048 sc1\n\t" \
  "global_load_lds_dwordx4 v162, s[42:43] offset:3072 sc1\n\ts_setprio 0\n\tv_mfma_f32_16x16x32_bf16 v[28:31], v[84:87], v[128:131], v[28:31]\n\t" \
  "s_setprio 3\n\tds_read_b64_tr_b16 v[84:85], v160 offset:32\n\tds_read_b64_tr_b16 v[86:87], v160 offset:1312\n\ts_setprio 0\n\t" \
  "v_mfma_f32_16x16x32_bf16 v[68:71], v[96:99], v[120:123], v[68:71]\n\ts_setprio 3\n\tglobal_load_dwordx4 v[120:123], v149, s[44:45] sc1\n\t" \
  "s_setprio 0\n\tv_mfma_f32_16x16x32_bf16 v[56:59], v[92:95], v[124:127], v[56:59]\n\ts_setprio 3\n\ts_add_u32 m0, m0, 1024\n\t" \
  "s_add_u32 s42, s42, 1024\n\ts_addc_u32 s43, s43, 0\n\tglobal_load_lds_dwordx4 v162, s[42:43] offset:3072 sc1\n\ts_setprio 0\n\t" \
  "v_mfma_f32_16x16x32_bf16 v[44:47], v[88:91], v[128:131], v[44:47]\n\ts_setprio 3\n\tds_read_b64_tr_b16 v[88:89], v160 offset:64\n\t" \
  "ds_read_b64_tr_b16 v[90:91], v160 offset:1344\n\ts_add_u32 s42, s42, 4096\n\ts_addc_u32 s43, s43, 0\n\ts_setprio 0\n\t" \
  "v_mfma_f32_16x16x32_bf16 v[72:75], v[96:99], v[124:127], v[72:75]\n\ts_setprio 3\n\tglobal_load_dwordx4 v[124:127], v150, s[44:45] sc1\n\t" \
  "s_setprio 0\n\tv_mfma_f32_16x16x32_bf16 v[60:63], v[92:95], v[128:131], v[60:63]\n\ts_setprio 3\n\tds_read_b64_tr_b16 v[92:93], v160 offset:96\n\t" \
  "ds_read_b64_tr_b16 v[94:95], v160 offset:1376\n\ts_setprio 0\n\tv_mfma_f32_16x16x32_bf16 v[76:79], v[96:99], v[128:131], v[76:79]\n\ts_setprio 3\n\t" \
  "ds_read_b64_tr_b16 v[96:97], v160 offset:128\n\tds_read_b64_tr_b16 v[98:99], v160 offset:1408\n\t" \
  "global_load_dwordx4 v[128:131], v151, s[44:45] sc1\n\ts_add_u32 s44, s44, 1024\n\ts_addc_u32 s45, s45, 0\n\ts_cmp_eq_u32 %[t0], 5\n\t" \
  "s_cbranch_scc1 .Lfc%=\n\t.Lhc%=:\n\ts_add_u32 %[t0], %[t0], 1\n\ts_waitcnt vmcnt(18)\n\ts_waitcnt lgkmcnt(8)\n\ts_setprio 0\n\t" \
  "v_mfma_f32_16x16x32_bf16 v[0:3], v[80:83], v[132:135], v[0:3]\n\ts_waitcnt lgkmcnt(6)\n\t" \
  "v_mfma_f32_16x16x32_bf16 v[16:19], v[84:87], v[132:135], v[16:19]\n\tv_mfma_f32_16x16x32_bf16 v[4:7], v[80:83], v[136:139], v[4:7]\n\t" \
  "s_waitcnt lgkmcnt(4)\n\tv_mfma_f32_16x16x32_bf16 v[32:35], v[88:91], v[132:135], v[32:35]\n\t" \
  "v_mfma_f32_16x16x32_bf16 v[20:23], v[84:87], v[136:139], v[20:23]\n\tv_mfma_f32_16x16x32_bf16 v[8:11], v[80:83], v[140:143], v[8:11]\n\t" \
  "s_waitcnt lgkmcnt(2)\n\tv_mfma_f32_16x16x32_bf16 v[48:51], v[92:95], v[132:135], v[48:51]\n\t" \
  "v_mfma_f32_16x16x32_bf16 v[36:39], v[88:91], v[136:139], v[36:39]\n\ts_setprio 3\n\ts_add_u32 %[t1], %[ro], 5120\n\ts_cmp_eq_u32 %[t1], 25600\n\t" \
  "s_cselect_b32 %[t1], 0, %[t1]\n\tv_add_u32 v160, %[t1], v152\n\ts_setprio 0\n\tv_mfma_f32_16x16x32_bf16 v[24:27], v[84:87], v[140:143], v[24:27]\n\t" \
  "v_mfma_f32_16x16x32_bf16 v[12:15], v[80:83], v[144:147], v[12:15]\n\ts_setprio 3\n\tds_read_b64_tr_b16 v[80:81], v160 offset:0\n\t" \
  "ds_read_b64_tr_b16 v[82:83], v160 offset:1280\n\ts_waitcnt lgkmcnt(2)\n\ts_setprio 0\n\t" \
  "v_mfma_f32_16x16x32_bf16 v[64:67], v[96:99], v[132:135], v[64:67]\n\ts_setprio 3\n\tglobal_load_dwordx4 v[132:135], v148, s[44:45] sc1\n\t" \
  "s_setprio 0\n\tv_mfma_f32_16x16x32_bf16 v[52:55], v[92:95], v[136:139], v[52:55]\n\ts_setprio 3\n\ts_add_u32 m0, %[ring], %[ro]\n\t" \
  "s_mov_b32 %[ro], %[t1]\n\tglobal_load_lds_dwordx4 v162, s[42:43] sc1\n\tglobal_load_lds_dwordx4 v162, s[42:43] offset:1024 sc1\n\ts_setprio 0\n\t" \
  "v_mfma_f32_16x16x32_bf16 v[40:43], v[88:91], v[140:143], v[40:43]\n\ts_setprio 3\n\tglobal_load_lds_dwordx4 v162, s[42:43] offset:2048 sc1\n\t" \
  "global_load_lds_dwordx4 v162, s[42:43] offset:3072 sc1\n\ts_setprio 0\n\tv_mfma_f32_16x16x32_bf16 v[28:31], v[84:87], v[144:147], v[28:31]\n\t" \
  "s_setprio 3\n\tds_read_b64_tr_b16 v[84:85], v160 offset:32\n\tds_read_b64_tr_b16 v[86:87], v160 offset:1312\n\ts_setprio 0\n\t" \
  "v_mfma_f32_16x16x32_bf16 v[68:71], v[96:99], v[136:139], v[68:71]\n\ts_setprio 3\n\tglobal_load_dwordx4 v[136:139], v149, s[44:45] sc1\n\t" \
  "s_setprio 0\n\tv_mfma_f32_16x16x32_bf16 v[56:59], v[92:95], v[140:143], v[56:59]\n\ts_setprio 3\n\ts_add_u32 m0, m0, 1024\n\t" \
  "s_add_u32 s42, s42, 1024\n\ts_addc_u32 s43, s43, 0\n\tglobal_load_lds_dwordx4 v162, s[42:43] offset:3072 sc1\n\ts_setprio 0\n\t" \
  "v_mfma_f32_16x16x32_bf16 v[44:47], v[88:91], v[144:147], v[44:47]\n\ts_setprio 3\n\tds_read_b64_tr_b16 v[88:89], v160 offset:64\n\t" \
  "ds_read_b64_tr_b16 v[90:91], v160 offset:1344\n\ts_add_u32 s42, s42, 4096\n\ts_addc_u32 s43, s43, 0\n\ts_setprio 0\n\t" \
  "v_mfma_f32_16x16x32_bf16 v[72:75], v[96:99], v[140:143], v[72:75]\n\ts_setprio 3\n\tglobal_load_dwordx4 v[140:143], v150, s[44:45] sc1\n\t" \
  "s_setprio 0\n\tv_mfma_f32_16x16x32_bf16 v[60:63], v[92:95], v[144:147], v[60:63]\n\ts_setprio 3\n\tds_read_b64_tr_b16 v[92:93], v160 offset:96\n\t" \
  "ds_read_b64_tr_b16 v[94:95], v160 offset:1376\n\ts_setprio 0\n\tv_mfma_f32_16x16x32_bf16 v[76:79], v[96:99], v[144:147], v[76:79]\n\ts_setprio 3\n\t" \
  "ds_read_b64_tr_b16 v[96:97], v160 offset:128\n\tds_read_b64_tr_b16 v[98:99], v160 offset:1408\n\t" \
  "global_load_dwordx4 v[144:147], v151, s[44:45] sc1\n\ts_add_u32 s44, s44, 1024\n\ts_addc_u32 s45, s45, 0\n\ts_cmp_lg_u32 %[t0], 9\n\t" \
  "s_cbranch_scc1 .Ldl%=\n\ts_cmp_eq_u32 9, 5\n\ts_cbranch_scc1 .Lfd%=\n\t.Lhd%=:\n\ts_waitcnt vmcnt(18)\n\ts_waitcnt lgkmcnt(8)\n\t" \
  "s_setprio 0\n\tv_mfma_f32_16x16x32_bf16 v[0:3], v[80:83], v[100:103], v[0:3]\n\ts_waitcnt lgkmcnt(6)\n\t" \
  "v_mfma_f32_16x16x32_bf16 v[16:19], v[84:87], v[100:103], v[16:19]\n\tv_mfma_f32_16x16x32_bf16 v[4:7], v[80:83], v[104:107], v[4:7]\n\t" \
  "s_waitcnt lgkmcnt(4)\n\tv_mfma_f32_16x16x32_bf16 v[32:35], v[88:91], v[100:103], v[32:35]\n\t" \
  "v_mfma_f32_16x16x32_bf16 v[20:23], v[84:87], v[104:107], v[20:23]\n\tv_mfma_f32_16x16x32_bf16 v[8:11], v[80:83], v[108:111], v[8:11]\n\t" \
  "s_waitcnt lgkmcnt(2)\n\tv_mfma_f32_16x16x32_bf16 v[48:51], v[92:95], v[100:103], v[48:51]\n\t" \
  "v_mfma_f32_16x16x32_bf16 v[36:39], v[88:91], v[104:107], v[36:39]\n\ts_setprio 3\n\ts_add_u32 %[t1], %[ro], 5120\n\ts_cmp_eq_u32 %[t1], 25600\n\t" \
  "s_cselect_b32 %[t1], 0, %[t1]\n\tv_add_u32 v160, %[t1], v152\n\ts_setprio 0\n\tv_mfma_f32_16x16x32_bf16 v[24:27], v[84:87], v[108:111], v[24:27]\n\t" \
  "v_mfma_f32_16x16x32_bf16 v[12:15], v[80:83], v[112:115], v[12:15]\n\ts_setprio 3\n\tds_read_b64_tr_b16 v[80:81], v160 offset:0\n\t" \
  "ds_read_b64_tr_b16 v[82:83], v160 offset:1280\n\ts_waitcnt lgkmcnt(2)\n\ts_setprio 0\n\t" \
  "v_mfma_f32_16x16x32_bf16 v[64:67], v[96:99], v[100:103], v[64:67]\n\tv_mfma_f32_16x16x32_bf16 v[52:55], v[92:95], v[104:107], v[52:55]\n\t" \
  "s_setprio 3\n\ts_add_u32 m0, %[ring], %[ro]\n\ts_mov_b32 %[ro], %[t1]\n\tglobal_load_lds_dwordx4 v162, s[42:43] sc1\n\t" \
  "global_load_lds_dwordx4 v162, s[42:43] offset:1024 sc1\n\ts_setprio 0\n\tv_mfma_f32_16x16x32_bf16 v[40:43], v[88:91], v[108:111], v[40:43]\n\t" \
  "s_setprio 3\n\tglobal_load_lds_dwordx4 v162, s[42:43] offset:2048 sc1\n\tglobal_load_lds_dwordx4 v162, s[42:43] offset:3072 sc1\n\ts_setprio 0\n\t" \
  "v_mfma_f32_16x16x32_bf16 v[28:31], v[84:87], v[112:115], v[28:31]\n\ts_setprio 3\n\tds_read_b64_tr_b16 v[84:85], v160 offset:32\n\t" \
  "ds_read_b64_tr_b16 v[86:87], v160 offset:1312\n\ts_setprio 0\n\tv_mfma_f32_16x16x32_bf16 v[68:71], v[96:99], v[104:107], v[68:71]\n\t" \
  "v_mfma_f32_16x16x32_bf16 v[56:59], v[92:95], v[108:111], v[56:59]\n\ts_setprio 3\n\ts_add_u32 m0, m0, 1024\n\ts_add_u32 s42, s42, 1024\n\t" \
  "s_addc_u32 s43, s43, 0\n\tglobal_load_lds_dwordx4 v162, s[42:43] offset:3072 sc1\n\ts_setprio 0\n\t" \
  "v_mfma_f32_16x16x32_bf16 v[44:47], v[88:91], v[112:115], v[44:47]\n\ts_setprio 3\n\tds_read_b64_tr_b16 v[88:89], v160 offset:64\n\t" \
  "ds_read_b64_tr_b16 v[90:91], v160 offset:1344\n\ts_add_u32 s42, s42, 4096\n\ts_addc_u32 s43, s43, 0\n\ts_setprio 0\n\t" \
  "v_mfma_f32_16x16x32_bf16 v[72:75], v[96:99], v[108:111], v[72:75]\n\tv_mfma_f32_16x16x32_bf16 v[60:63], v[92:95], v[112:115], v[60:63]\n\t" \
  "s_setprio 3\n\tds_read_b64_tr_b16 v[92:93], v160 offset:96\n\tds_read_b64_tr_b16 v[94:95], v160 offset:1376\n\ts_setprio 0\n\t" \
  "v_mfma_f32_16x16x32_bf16 v[76:79], v[96:99], v[112:115], v[76:79]\n\ts_setprio 3\n\tds_read_b64_tr_b16 v[96:97], v160 offset:128\n\t" \
  "ds_read_b64_tr_b16 v[98:99], v160 offset:1408\n\ts_waitcnt vmcnt(14)\n\ts_waitcnt lgkmcnt(8)\n\ts_setprio 0\n\t" \
  "v_mfma_f32_16x16x32_bf16 v[0:3], v[80:83], v[116:119], v[0:3]\n\ts_waitcnt lgkmcnt(6)\n\t" \
  "v_mfma_f32_16x16x32_bf16 v[16:19], v[84:87], v[116:119], v[16:19]\n\tv_mfma_f32_16x16x32_bf16 v[4:7], v[80:83], v[120:123], v[4:7]\n\t" \
  "s_waitcnt lgkmcnt(4)\n\tv_mfma_f32_16x16x32_bf16 v[32:35], v[88:91], v[116:119], v[32:35]\n\t" \
  "v_mfma_f32_16x16x32_bf16 v[20:23], v[84:87], v[120:123], v[20:23]\n\tv_mfma_f32_16x16x32_bf16 v[8:11], v[80:83], v[124:127], v[8:11]\n\t" \
  "s_waitcnt lgkmcnt(2)\n\tv_mfma_f32_16x16x32_bf16 v[48:51], v[92:95], v[116:119], v[48:51]\n\t" \
  "v_mfma_f32_16x16x32_bf16 v[36:39], v[88:91], v[120:123], v[36:39]\n\ts_setprio 3\n\ts_add_u32 %[t1], %[ro], 5120\n\ts_cmp_eq_u32 %[t1], 25600\n\t" \
  "s_cselect_b32 %[t1], 0, %[t1]\n\tv_add_u32 v160, %[t1], v152\n\ts_setprio 0\n\tv_mfma_f32_16x16x32_bf16 v[24:27], v[84:87], v[124:127], v[24:27]\n\t" \
  "v_mfma_f32_16x16x32_bf16 v[12:15], v[80:83], v[128:131], v[12:15]\n\ts_setprio 3\n\tds_read_b64_tr_b16 v[80:81], v160 offset:0\n\t" \
  "ds_read_b64_tr_b16 v[82:83], v160 offset:1280\n\ts_waitcnt lgkmcnt(2)\n\ts_setprio 0\n\t" \
  "v_mfma_f32_16x16x32_bf16 v[64:67], v[96:99], v[116:119], v[64:67]\n\tv_mfma_f32_16x16x32_bf16 v[52:55], v[92:95], v[120:123], v[52:55]\n\t" \
  "s_setprio 3\n\ts_add_u32 m0, %[ring], %[ro]\n\ts_mov_b32 %[ro], %[t1]\n\tglobal_load_lds_dwordx4 v162, s[42:43] sc1\n\t" \
  "global_load_lds_dwordx4 v162, s[42:43] offset:1024 sc1\n\ts_setprio 0\n\tv_mfma_f32_16x16x32_bf16 v[40:43], v[88:91], v[124:127], v[40:43]\n\t" \
  "s_setprio 3\n\tglobal_load_lds_dwordx4 v162, s[42:43] offset:2048 sc1\n\tglobal_load_lds_dwordx4 v162, s[42:43] offset:3072 sc1\n\ts_setprio 0\n\t" \
  "v_mfma_f32_16x16x32_bf16 v[28:31], v[84:87], v[128:131], v[28:31]\n\ts_setprio 3\n\tds_read_b64_tr_b16 v[84:85], v160 offset:32\n\t" \
  "ds_read_b64_tr_b16 v[86:87], v160 offset:1312\n\ts_setprio 0\n\tv_mfma_f32_16x16x32_bf16 v[68:71], v[96:99], v[120:123], v[68:71]\n\t" \
  "v_mfma_f32_16x16x32_bf16 v[56:59], v[92:95], v[124:127], v[56:59]\n\ts_setprio 3\n\ts_add_u32 m0, m0, 1024\n\ts_add_u32 s42, s42, 1024\n\t" \
  "s_addc_u32 s43, s43, 0\n\tglobal_load_lds_dwordx4 v162, s[42:43] offset:3072 sc1\n\ts_setprio 0\n\t" \
  "v_mfma_f32_16x16x32_bf16 v[44:47], v[88:91], v[128:131], v[44:47]\n\ts_setprio 3\n\tds_read_b64_tr_b16 v[88:89], v160 offset:64\n\t" \
  "ds_read_b64_tr_b16 v[90:91], v160 offset:1344\n\ts_add_u32 s42, s42, 4096\n\ts_addc_u32 s43, s43, 0\n\ts_setprio 0\n\t" \
  "v_mfma_f32_16x16x32_bf16 v[72:75], v[96:99], v[124:127], v[72:75]\n\tv_mfma_f32_16x16x32_bf16 v[60:63], v[92:95], v[128:131], v[60:63]\n\t" \
  "s_setprio 3\n\tds_read_b64_tr_b16 v[92:93], v160 offset:96\n\tds_read_b64_tr_b16 v[94:95], v160 offset:1376\n\ts_setprio 0\n\t" \
  "v_mfma_f32_16x16x32_bf16 v[76:79], v[96:99], v[128:131], v[76:79]\n\ts_setprio 3\n\tds_read_b64_tr_b16 v[96:97], v160 offset:128\n\t" \
  "ds_read_b64_tr_b16 v[98:99], v160 offset:1408\n\ts_waitcnt vmcnt(10)\n\ts_waitcnt lgkmcnt(8)\n\ts_setprio 0\n\t" \
  "v_mfma_f32_16x16x32_bf16 v[0:3], v[80:83], v[132:135], v[0:3]\n\ts_waitcnt lgkmcnt(6)\n\t" \
  "v_mfma_f32_16x16x32_bf16 v[16:19], v[84:87], v[132:135], v[16:19]\n\tv_mfma_f32_16x16x32_bf16 v[4:7], v[80:83], v[136:139], v[4:7]\n\ts_setprio 3\n\t" \
  "s_setprio 3\n\tglobal_load_dword v164, v163, %[of] sc1\n\ts_setprio 0\n\ts_waitcnt lgkmcnt(4)\n\ts_setprio 0\n\t" \
  "v_mfma_f32_16x16x32_bf16 v[32:35], v[88:91], v[132:135], v[32:35]\n\tv_mfma_f32_16x16x32_bf16 v[20:23], v[84:87], v[136:139], v[20:23]\n\t" \
  "v_mfma_f32_16x16x32_bf16 v[8:11], v[80:83], v[140:143], v[8:11]\n\ts_waitcnt lgkmcnt(2)\n\t" \
  "v_mfma_f32_16x16x32_bf16 v[48:51], v[92:95], v[132:135], v[48:51]\n\tv_mfma_f32_16x16x32_bf16 v[36:39], v[88:91], v[136:139], v[36:39]\n\t" \
  "s_setprio 3\n\ts_add_u32 %[t1], %[ro], 5120\n\ts_cmp_eq_u32 %[t1], 25600\n\ts_cselect_b32 %[t1], 0, %[t1]\n\tv_add_u32 v160, %[t1], v152\n\t" \
  "s_setprio 0\n\tv_mfma_f32_16x16x32_bf16 v[24:27], v[84:87], v[140:143], v[24:27]\n\t" \
  "v_mfma_f32_16x16x32_bf16 v[12:15], v[80:83], v[144:147], v[12:15]\n\ts_setprio 3\n\tds_read_b64_tr_b16 v[80:81], v160 offset:0\n\t" \
  "ds_read_b64_tr_b16 v[82:83], v160 offset:1280\n\ts_waitcnt lgkmcnt(2)\n\ts_setprio 0\n\t" \
  "v_mfma_f32_16x16x32_bf16 v[64:67], v[96:99], v[132:135], v[64:67]\n\tv_mfma_f32_16x16x32_bf16 v[52:55], v[92:95], v[136:139], v[52:55]\n\t" \
  "s_setprio 3\n\ts_add_u32 m0, %[ring], %[ro]\n\ts_mov_b32 %[ro], %[t1]\n\tglobal_load_lds_dwordx4 v162, s[42:43] sc1\n\t" \
  "global_load_lds_dwordx4 v162, s[42:43] offset:1024 sc1\n\ts_setprio 0\n\tv_mfma_f32_16x16x32_bf16 v[40:43], v[88:91], v[140:143], v[40:43]\n\t" \
  "s_setprio 3\n\tglobal_load_lds_dwordx4 v162, s[42:43] offset:2048 sc1\n\tglobal_load_lds_dwordx4 v162, s[42:43] offset:3072 sc1\n\ts_setprio 0\n\t" \
  "v_mfma_f32_16x16x32_bf16 v[28:31], v[84:87], v[144:147], v[28:31]\n\ts_setprio 3\n\tds_read_b64_tr_b16 v[84:85], v160 offset:32\n\t" \
  "ds_read_b64_tr_b16 v[86:87], v160 offset:1312\n\ts_setprio 0\n\tv_mfma_f32_16x16x32_bf16 v[68:71], v[96:99], v[136:139], v[68:71]\n\t" \
  "v_mfma_f32_16x16x32_bf16 v[56:59], v[92:95], v[140:143], v[56:59]\n\ts_setprio 3\n\ts_add_u32 m0, m0, 1024\n\ts_add_u32 s42, s42, 1024\n\t" \
  "s_addc_u32 s43, s43, 0\n\tglobal_load_lds_dwordx4 v162, s[42:43] offset:3072 sc1\n\ts_setprio 0\n\t" \
  "v_mfma_f32_16x16x32_bf16 v[44:47], v[88:91], v[144:147], v[44:47]\n\ts_setprio 3\n\tds_read_b64_tr_b16 v[88:89], v160 offset:64\n\t" \
  "ds_read_b64_tr_b16 v[90:91], v160 offset:1344\n\ts_add_u32 s42, s42, 4096\n\ts_addc_u32 s43, s43, 0\n\ts_setprio 0\n\t" \
  "v_mfma_f32_16x16x32_bf16 v[72:75], v[96:99], v[140:143], v[72:75]\n\tv_mfma_f32_16x16x32_bf16 v[60:63], v[92:95], v[144:147], v[60:63]\n\t" \
  "s_setprio 3\n\tds_read_b64_tr_b16 v[92:93], v160 offset:96\n\tds_read_b64_tr_b16 v[94:95], v160 offset:1376\n\ts_setprio 0\n\t" \
  "v_mfma_f32_16x16x32_bf16 v[76:79], v[96:99], v[144:147], v[76:79]\n\ts_setprio 3\n\tds_read_b64_tr_b16 v[96:97], v160 offset:128\n\t" \
  "ds_read_b64_tr_b16 v[98:99], v160 offset:1408\n\tds_read_b32 v100, v165 offset:0\n\tds_read_b32 v101, v165 offset:128\n\t" \
  "ds_read_b32 v102, v165 offset:256\n\tds_read_b32 v103, v165 offset:384\n\ts_waitcnt lgkmcnt(0)\n\ts_nop 3\n\t" \
  "v_pk_mul_f32 v[0:1], v[0:1], v[100:101] op_sel:[0,0] op_sel_hi:[1,0]\n\tv_pk_mul_f32 v[2:3], v[2:3], v[100:101] op_sel:[0,0] op_sel_hi:[1,0]\n\t" \
  "v_pk_mul_f32 v[4:5], v[4:5], v[100:101] op_sel:[0,1] op_sel_hi:[1,1]\n\tv_pk_mul_f32 v[6:7], v[6:7], v[100:101] op_sel:[0,1] op_sel_hi:[1,1]\n\t" \
  "v_pk_mul_f32 v[8:9], v[8:9], v[102:103] op_sel:[0,0] op_sel_hi:[1,0]\n\tv_pk_mul_f32 v[10:11], v[10:11], v[102:103] op_sel:[0,0] op_sel_hi:[1,0]\n\t" \
  "v_pk_mul_f32 v[12:13], v[12:13], v[102:103] op_sel:[0,1] op_sel_hi:[1,1]\n\t" \
  "v_pk_mul_f32 v[14:15], v[14:15], v[102:103] op_sel:[0,1] op_sel_hi:[1,1]\n\t" \
  "v_pk_mul_f32 v[16:17], v[16:17], v[100:101] op_sel:[0,0] op_sel_hi:[1,0]\n\t" \
  "v_pk_mul_f32 v[18:19], v[18:19], v[100:101] op_sel:[0,0] op_sel_hi:[1,0]\n\t" \
  "v_pk_mul_f32 v[20:21], v[20:21], v[100:101] op_sel:[0,1] op_sel_hi:[1,1]\n\t" \
  "v_pk_mul_f32 v[22:23], v[22:23], v[100:101] op_sel:[0,1] op_sel_hi:[1,1]\n\t" \
  "v_pk_mul_f32 v[24:25], v[24:25], v[102:103] op_sel:[0,0] op_sel_hi:[1,0]\n\t" \
  "v_pk_mul_f32 v[26:27], v[26:27], v[102:103] op_sel:[0,0] op_sel_hi:[1,0]\n\t" \
  "v_pk_mul_f32 v[28:29], v[28:29], v[102:103] op_sel:[0,1] op_sel_hi:[1,1]\n\t" \
  "v_pk_mul_f32 v[30:31], v[30:31], v[102:103] op_sel:[0,1] op_sel_hi:[1,1]\n\t" \
  "v_pk_mul_f32 v[32:33], v[32:33], v[100:101] op_sel:[0,0] op_sel_hi:[1,0]\n\t" \
  "v_pk_mul_f32 v[34:35], v[34:35], v[100:101] op_sel:[0,0] op_sel_hi:[1,0]\n\t" \
  "v_pk_mul_f32 v[36:37], v[36:37], v[100:101] op_sel:[0,1] op_sel_hi:[1,1]\n\t" \
  "v_pk_mul_f32 v[38:39], v[38:39], v[100:101] op_sel:[0,1] op_sel_hi:[1,1]\n\t" \
  "v_pk_mul_f32 v[40:41], v[40:41], v[102:103] op_sel:[0,0] op_sel_hi:[1,0]\n\t" \
  "v_pk_mul_f32 v[42:43], v[42:43], v[102:103] op_sel:[0,0] op_sel_hi:[1,0]\n\t" \
  "v_pk_mul_f32 v[44:45], v[44:45], v[102:103] op_sel:[0,1] op_sel_hi:[1,1]\n\t" \
  "v_pk_mul_f32 v[46:47], v[46:47], v[102:103] op_sel:[0,1] op_sel_hi:[1,1]\n\t" \
  "v_pk_mul_f32 v[48:49], v[48:49], v[100:101] op_sel:[0,0] op_sel_hi:[1,0]\n\t" \
  "v_pk_mul_f32 v[50:51], v[50:51], v[100:101] op_sel:[0,0] op_sel_hi:[1,0]\n\t" \
  "v_pk_mul_f32 v[52:53], v[52:53], v[100:101] op_sel:[0,1] op_sel_hi:[1,1]\n\t" \
  "v_pk_mul_f32 v[54:55], v[54:55], v[100:101] op_sel:[0,1] op_sel_hi:[1,1]\n\t" \
  "v_pk_mul_f32 v[56:57], v[56:57], v[102:103] op_sel:[0,0] op_sel_hi:[1,0]\n\t" \
  "v_pk_mul_f32 v[58:59], v[58:59], v[102:103] op_sel:[0,0] op_sel_hi:[1,0]\n\t" \
  "v_pk_mul_f32 v[60:61], v[60:61], v[102:103] op_sel:[0,1] op_sel_hi:[1,1]\n\t" \
  "v_pk_mul_f32 v[62:63], v[62:63], v[102:103] op_sel:[0,1] op_sel_hi:[1,1]\n\t" \
  "v_pk_mul_f32 v[64:65], v[64:65], v[100:101] op_sel:[0,0] op_sel_hi:[1,0]\n\t" \
  "v_pk_mul_f32 v[66:67], v[66:67], v[100:101] op_sel:[0,0] op_sel_hi:[1,0]\n\t" \
  "v_pk_mul_f32 v[68:69], v[68:69], v[100:101] op_sel:[0,1] op_sel_hi:[1,1]\n\t" \
  "v_pk_mul_f32 v[70:71], v[70:71], v[100:101] op_sel:[0,1] op_sel_hi:[1,1]\n\t" \
  "v_pk_mul_f32 v[72:73], v[72:73], v[102:103] op_sel:[0,0] op_sel_hi:[1,0]\n\t" \
  "v_pk_mul_f32 v[74:75], v[74:75], v[102:103] op_sel:[0,0] op_sel_hi:[1,0]\n\t" \
  "v_pk_mul_f32 v[76:77], v[76:77], v[102:103] op_sel:[0,1] op_sel_hi:[1,1]\n\t" \
  "v_pk_mul_f32 v[78:79], v[78:79], v[102:103] op_sel:[0,1] op_sel_hi:[1,1]\n\ts_mov_b32 %[t0], 0x20000\n\ts_waitcnt vmcnt(5)\n\t.Lpl%=:\n\t" \
  "v_cmp_ne_u32 vcc, %[tag], v164\n\ts_cbranch_vccz .Lpx%=\n\ts_sleep 2\n\tglobal_load_dword v164, v163, %[of] sc1\n\ts_sub_u32 %[t0], %[t0], 1\n\t" \
  "s_waitcnt vmcnt(0)\n\ts_cbranch_scc0 .Lpl%=\n\t.Lpx%=:\n\tglobal_load_dwordx4 v[100:103], v162, %[o0] offset:-2048 sc1\n\t" \
  "global_load_dwordx4 v[104:107], v162, %[o1] offset:-2048 sc1\n\tglobal_load_dwordx4 v[108:111], v162, %[o2] offset:-2048 sc1\n\t" \
  "global_load_dwordx4 v[112:115], v162, %[o3] offset:-2048 sc1\n\tglobal_load_dwordx4 v[116:119], v162, %[o0] offset:-1024 sc1\n\t" \
  "global_load_dwordx4 v[120:123], v162, %[o1] offset:-1024 sc1\n\tglobal_load_dwordx4 v[124:127], v162, %[o2] offset:-1024 sc1\n\t" \
  "global_load_dwordx4 v[128:131], v162, %[o3] offset:-1024 sc1\n\tglobal_load_dwordx4 v[132:135], v162, %[o0] offset:0 sc1\n\t" \
  "global_load_dwordx4 v[136:139], v162, %[o1] offset:0 sc1\n\tglobal_load_dwordx4 v[140:143], v162, %[o2] offset:0 sc1\n\t" \
  "global_load_dwordx4 v[144:147], v162, %[o3] offset:0 sc1\n\ts_add_u32 %[t1], %[ro], 5120\n\ts_cmp_eq_u32 %[t1], 25600\n\t" \
  "s_cselect_b32 %[t1], 0, %[t1]\n\tv_add_u32 v160, %[t1], v152\n\ts_mov_b32 %[ro], %[t1]\n\ts_waitcnt vmcnt(8)\n\t" \
  "v_mfma_f32_16x16x32_bf16 v[0:3], v[80:83], v[100:103], v[0:3]\n\tv_mfma_f32_16x16x32_bf16 v[4:7], v[80:83], v[104:107], v[4:7]\n\t" \
  "v_mfma_f32_16x16x32_bf16 v[8:11], v[80:83], v[108:111], v[8:11]\n\tv_mfma_f32_16x16x32_bf16 v[12:15], v[80:83], v[112:115], v[12:15]\n\t" \
  "ds_read_b64_tr_b16 v[80:81], v160 offset:0\n\tds_read_b64_tr_b16 v[82:83], v160 offset:1280\n\t" \
  "v_mfma_f32_16x16x32_bf16 v[16:19], v[84:87], v[100:103], v[16:19]\n\tv_mfma_f32_16x16x32_bf16 v[20:23], v[84:87], v[104:107], v[20:23]\n\t" \
  "v_mfma_f32_16x16x32_bf16 v[24:27], v[84:87], v[108:111], v[24:27]\n\tv_mfma_f32_16x16x32_bf16 v[28:31], v[84:87], v[112:115], v[28:31]\n\t" \
  "ds_read_b64_tr_b16 v[84:85], v160 offset:32\n\tds_read_b64_tr_b16 v[86:87], v160 offset:1312\n\t" \
  "v_mfma_f32_16x16x32_bf16 v[32:35], v[88:91], v[100:103], v[32:35]\n\tv_mfma_f32_16x16x32_bf16 v[36:39], v[88:91], v[104:107], v[36:39]\n\t" \
  "v_mfma_f32_16x16x32_bf16 v[40:43], v[88:91], v[108:111], v[40:43]\n\tv_mfma_f32_16x16x32_bf16 v[44:47], v[88:91], v[112:115], v[44:47]\n\t" \
  "ds_read_b64_tr_b16 v[88:89], v160 offset:64\n\tds_read_b64_tr_b16 v[90:91], v160 offset:1344\n\t" \
  "v_mfma_f32_16x16x32_bf16 v[48:51], v[92:95], v[100:103], v[48:51]\n\tv_mfma_f32_16x16x32_bf16 v[52:55], v[92:95], v[104:107], v[52:55]\n\t" \
  "v_mfma_f32_16x16x32_bf16 v[56:59], v[92:95], v[108:111], v[56:59]\n\tv_mfma_f32_16x16x32_bf16 v[60:63], v[92:95], v[112:115], v[60:63]\n\t" \
  "ds_read_b64_tr_b16 v[92:93], v160 offset:96\n\tds_read_b64_tr_b16 v[94:95], v160 offset:1376\n\t" \
  "v_mfma_f32_16x16x32_bf16 v[64:67], v[96:99], v[100:103], v[64:67]\n\tv_mfma_f32_16x16x32_bf16 v[68:71], v[96:99], v[104:107], v[68:71]\n\t" \
  "v_mfma_f32_16x16x32_bf16 v[72:75], v[96:99], v[108:111], v[72:75]\n\tv_mfma_f32_16x16x32_bf16 v[76:79], v[96:99], v[112:115], v[76:79]\n\t" \
  "ds_read_b64_tr_b16 v[96:97], v160 offset:128\n\tds_read_b64_tr_b16 v[98:99], v160 offset:1408\n\t" \
  "global_load_dwordx4 v[100:103], v162, %[o0] offset:1024 sc1\n\tglobal_load_dwordx4 v[104:107], v162, %[o1] offset:1024 sc1\n\t" \
  "global_load_dwordx4 v[108:111], v162, %[o2] offset:1024 sc1\n\tglobal_load_dwordx4 v[112:115], v162, %[o3] offset:1024 sc1\n\t" \
  "s_add_u32 %[t1], %[ro], 5120\n\ts_cmp_eq_u32 %[t1], 25600\n\ts_cselect_b32 %[t1], 0, %[t1]\n\tv_add_u32 v160, %[t1], v152\n\t" \
  "s_mov_b32 %[ro], %[t1]\n\ts_waitcnt vmcnt(8)\n\ts_waitcnt lgkmcnt(8)\n\tv_mfma_f32_16x16x32_bf16 v[0:3], v[80:83], v[116:119], v[0:3]\n\t" \
  "v_mfma_f32_16x16x32_bf16 v[4:7], v[80:83], v[120:123], v[4:7]\n\tv_mfma_f32_16x16x32_bf16 v[8:11], v[80:83], v[124:127], v[8:11]\n\t" \
  "v_mfma_f32_16x16x32_bf16 v[12:15], v[80:83], v[128:131], v[12:15]\n\tds_read_b64_tr_b16 v[80:81], v160 offset:0\n\t" \
  "ds_read_b64_tr_b16 v[82:83], v160 offset:1280\n\ts_waitcnt lgkmcnt(8)\n\tv_mfma_f32_16x16x32_bf16 v[16:19], v[84:87], v[116:119], v[16:19]\n\t" \
  "v_mfma_f32_16x16x32_bf16 v[20:23], v[84:87], v[120:123], v[20:23]\n\tv_mfma_f32_16x16x32_bf16 v[24:27], v[84:87], v[124:127], v[24:27]\n\t" \
  "v_mfma_f32_16x16x32_bf16 v[28:31], v[84:87], v[128:131], v[28:31]\n\tds_read_b64_tr_b16 v[84:85], v160 offset:32\n\t" \
  "ds_read_b64_tr_b16 v[86:87], v160 offset:1312\n\ts_waitcnt lgkmcnt(8)\n\tv_mfma_f32_16x16x32_bf16 v[32:35], v[88:91], v[116:119], v[32:35]\n\t" \
  "v_mfma_f32_16x16x32_bf16 v[36:39], v[88:91], v[120:123], v[36:39]\n\tv_mfma_f32_16x16x32_bf16 v[40:43], v[88:91], v[124:127], v[40:43]\n\t" \
  "v_mfma_f32_16x16x32_bf16 v[44:47], v[88:91], v[128:131], v[44:47]\n\tds_read_b64_tr_b16 v[88:89], v160 offset:64\n\t" \
  "ds_read_b64_tr_b16 v[90:91], v160 offset:1344\n\ts_waitcnt lgkmcnt(8)\n\tv_mfma_f32_16x16x32_bf16 v[48:51], v[92:95], v[116:119], v[48:51]\n\t" \
  "v_mfma_f32_16x16x32_bf16 v[52:55], v[92:95], v[120:123], v[52:55]\n\tv_mfma_f32_16x16x32_bf16 v[56:59], v[92:95], v[124:127], v[56:59]\n\t" \
  "v_mfma_f32_16x16x32_bf16 v[60:63], v[92:95], v[128:131], v[60:63]\n\tds_read_b64_tr_b16 v[92:93], v160 offset:96\n\t" \
  "ds_read_b64_tr_b16 v[94:95], v160 offset:1376\n\ts_waitcnt lgkmcnt(8)\n\tv_mfma_f32_16x16x32_bf16 v[64:67], v[96:99], v[116:119], v[64:67]\n\t" \
  "v_mfma_f32_16x16x32_bf16 v[68:71], v[96:99], v[120:123], v[68:71]\n\tv_mfma_f32_16x16x32_bf16 v[72:75], v[96:99], v[124:127], v[72:75]\n\t" \
  "v_mfma_f32_16x16x32_bf16 v[76:79], v[96:99], v[128:131], v[76:79]\n\tds_read_b64_tr_b16 v[96:97], v160 offset:128\n\t" \
  "ds_read_b64_tr_b16 v[98:99], v160 offset:1408\n\tglobal_load_dwordx4 v[116:119], v162, %[o0] offset:2048 sc1\n\t" \
  "global_load_dwordx4 v[120:123], v162, %[o1] offset:2048 sc1\n\tglobal_load_dwordx4 v[124:127], v162, %[o2] offset:2048 sc1\n\t" \
  "global_load_dwordx4 v[128:131], v162, %[o3] offset:2048 sc1\n\ts_add_u32 %[t1], %[ro], 5120\n\ts_cmp_eq_u32 %[t1], 25600\n\t" \
  "s_cselect_b32 %[t1], 0, %[t1]\n\tv_add_u32 v160, %[t1], v152\n\ts_mov_b32 %[ro], %[t1]\n\ts_waitcnt vmcnt(8)\n\ts_waitcnt lgkmcnt(8)\n\t" \
  "v_mfma_f32_16x16x32_bf16 v[0:3], v[80:83], v[132:135], v[0:3]\n\tv_mfma_f32_16x16x32_bf16 v[4:7], v[80:83], v[136:139], v[4:7]\n\t" \
  "v_mfma_f32_16x16x32_bf16 v[8:11], v[80:83], v[140:143], v[8:11]\n\tv_mfma_f32_16x16x32_bf16 v[12:15], v[80:83], v[144:147], v[12:15]\n\t" \
  "ds_read_b64_tr_b16 v[80:81], v160 offset:0\n\tds_read_b64_tr_b16 v[82:83], v160 offset:1280\n\ts_waitcnt lgkmcnt(8)\n\t" \
  "v_mfma_f32_16x16x32_bf16 v[16:19], v[84:87], v[132:135], v[16:19]\n\tv_mfma_f32_16x16x32_bf16 v[20:23], v[84:87], v[136:139], v[20:23]\n\t" \
  "v_mfma_f32_16x16x32_bf16 v[24:27], v[84:87], v[140:143], v[24:27]\n\tv_mfma_f32_16x16x32_bf16 v[28:31], v[84:87], v[144:147], v[28:31]\n\t" \
  "ds_read_b64_tr_b16 v[84:85], v160 offset:32\n\tds_read_b64_tr_b16 v[86:87], v160 offset:1312\n\ts_waitcnt lgkmcnt(8)\n\t" \
  "v_mfma_f32_16x16x32_bf16 v[32:35], v[88:91], v[132:135], v[32:35]\n\tv_mfma_f32_16x16x32_bf16 v[36:39], v[88:91], v[136:139], v[36:39]\n\t" \
  "v_mfma_f32_16x16x32_bf16 v[40:43], v[88:91], v[140:143], v[40:43]\n\tv_mfma_f32_16x16x32_bf16 v[44:47], v[88:91], v[144:147], v[44:47]\n\t" \
  "ds_read_b64_tr_b16 v[88:89], v160 offset:64\n\tds_read_b64_tr_b16 v[90:91], v160 offset:1344\n\ts_waitcnt lgkmcnt(8)\n\t" \
  "v_mfma_f32_16x16x32_bf16 v[48:51], v[92:95], v[132:135], v[48:51]\n\tv_mfma_f32_16x16x32_bf16 v[52:55], v[92:95], v[136:139], v[52:55]\n\t" \
  "v_mfma_f32_16x16x32_bf16 v[56:59], v[92:95], v[140:143], v[56:59]\n\tv_mfma_f32_16x16x32_bf16 v[60:63], v[92:95], v[144:147], v[60:63]\n\t" \
  "ds_read_b64_tr_b16 v[92:93], v160 offset:96\n\tds_read_b64_tr_b16 v[94:95], v160 offset:1376\n\ts_waitcnt lgkmcnt(8)\n\t" \
  "v_mfma_f32_16x16x32_bf16 v[64:67], v[96:99], v[132:135], v[64:67]\n\tv_mfma_f32_16x16x32_bf16 v[68:71], v[96:99], v[136:139], v[68:71]\n\t" \
  "v_mfma_f32_16x16x32_bf16 v[72:75], v[96:99], v[140:143], v[72:75]\n\tv_mfma_f32_16x16x32_bf16 v[76:79], v[96:99], v[144:147], v[76:79]\n\t" \
  "ds_read_b64_tr_b16 v[96:97], v160 offset:128\n\tds_read_b64_tr_b16 v[98:99], v160 offset:1408\n\ts_add_u32 %[t1], %[ro], 5120\n\t" \
  "s_cmp_eq_u32 %[t1], 25600\n\ts_cselect_b32 %[t1], 0, %[t1]\n\tv_add_u32 v160, %[t1], v152\n\ts_mov_b32 %[ro], %[t1]\n\ts_waitcnt vmcnt(4)\n\t" \
  "s_waitcnt lgkmcnt(8)\n\tv_mfma_f32_16x16x32_bf16 v[0:3], v[80:83], v[100:103], v[0:3]\n\t" \
  "v_mfma_f32_16x16x32_bf16 v[4:7], v[80:83], v[104:107], v[4:7]\n\tv_mfma_f32_16x16x32_bf16 v[8:11], v[80:83], v[108:111], v[8:11]\n\t" \
  "v_mfma_f32_16x16x32_bf16 v[12:15], v[80:83], v[112:115], v[12:15]\n\tds_read_b64_tr_b16 v[80:81], v160 offset:0\n\t" \
  "ds_read_b64_tr_b16 v[82:83], v160 offset:1280\n\ts_waitcnt lgkmcnt(8)\n\tv_mfma_f32_16x16x32_bf16 v[16:19], v[84:87], v[100:103], v[16:19]\n\t" \
  "v_mfma_f32_16x16x32_bf16 v[20:23], v[84:87], v[104:107], v[20:23]\n\tv_mfma_f32_16x16x32_bf16 v[24:27], v[84:87], v[108:111], v[24:27]\n\t" \
  "v_mfma_f32_16x16x32_bf16 v[28:31], v[84:87], v[112:115], v[28:31]\n\tds_read_b64_tr_b16 v[84:85], v160 offset:32\n\t" \
  "ds_read_b64_tr_b16 v[86:87], v160 offset:1312\n\ts_waitcnt lgkmcnt(8)\n\tv_mfma_f32_16x16x32_bf16 v[32:35], v[88:91], v[100:103], v[32:35]\n\t" \
  "v_mfma_f32_16x16x32_bf16 v[36:39], v[88:91], v[104:107], v[36:39]\n\tv_mfma_f32_16x16x32_bf16 v[40:43], v[88:91], v[108:111], v[40:43]\n\t" \
  "v_mfma_f32_16x16x32_bf16 v[44:47], v[88:91], v[112:115], v[44:47]\n\tds_read_b64_tr_b16 v[88:89], v160 offset:64\n\t" \
  "ds_read_b64_tr_b16 v[90:91], v160 offset:1344\n\ts_waitcnt lgkmcnt(8)\n\tv_mfma_f32_16x16x32_bf16 v[48:51], v[92:95], v[100:103], v[48:51]\n\t" \
  "v_mfma_f32_16x16x32_bf16 v[52:55], v[92:95], v[104:107], v[52:55]\n\tv_mfma_f32_16x16x32_bf16 v[56:59], v[92:95], v[108:111], v[56:59]\n\t" \
  "v_mfma_f32_16x16x32_bf16 v[60:63], v[92:95], v[112:115], v[60:63]\n\tds_read_b64_tr_b16 v[92:93], v160 offset:96\n\t" \
  "ds_read_b64_tr_b16 v[94:95], v160 offset:1376\n\ts_waitcnt lgkmcnt(8)\n\tv_mfma_f32_16x16x32_bf16 v[64:67], v[96:99], v[100:103], v[64:67]\n\t" \
  "v_mfma_f32_16x16x32_bf16 v[68:71], v[96:99], v[104:107], v[68:71]\n\tv_mfma_f32_16x16x32_bf16 v[72:75], v[96:99], v[108:111], v[72:75]\n\t" \
  "v_mfma_f32_16x16x32_bf16 v[76:79], v[96:99], v[112:115], v[76:79]\n\tds_read_b64_tr_b16 v[96:97], v160 offset:128\n\t" \
  "ds_read_b64_tr_b16 v[98:99], v160 offset:1408\n\ts_waitcnt vmcnt(0)\n\ts_waitcnt lgkmcnt(8)\n\t" \
  "v_mfma_f32_16x16x32_bf16 v[0:3], v[80:83], v[116:119], v[0:3]\n\tv_mfma_f32_16x16x32_bf16 v[4:7], v[80:83], v[120:123], v[4:7]\n\t" \
  "v_mfma_f32_16x16x32_bf16 v[8:11], v[80:83], v[124:127], v[8:11]\n\tv_mfma_f32_16x16x32_bf16 v[12:15], v[80:83], v[128:131], v[12:15]\n\t" \
  "s_waitcnt lgkmcnt(6)\n\tv_mfma_f32_16x16x32_bf16 v[16:19], v[84:87], v[116:119], v[16:19]\n\t" \
  "v_mfma_f32_16x16x32_bf16 v[20:23], v[84:87], v[120:123], v[20:23]\n\tv_mfma_f32_16x16x32_bf16 v[24:27], v[84:87], v[124:127], v[24:27]\n\t" \
  "v_mfma_f32_16x16x32_bf16 v[28:31], v[84:87], v[128:131], v[28:31]\n\ts_waitcnt lgkmcnt(4)\n\t" \
  "v_mfma_f32_16x16x32_bf16 v[32:35], v[88:91], v[116:119], v[32:35]\n\tv_mfma_f32_16x16x32_bf16 v[36:39], v[88:91], v[120:123], v[36:39]\n\t" \
  "v_mfma_f32_16x16x32_bf16 v[40:43], v[88:91], v[124:127], v[40:43]\n\tv_mfma_f32_16x16x32_bf16 v[44:47], v[88:91], v[128:131], v[44:47]\n\t" \
  "s_waitcnt lgkmcnt(2)\n\tv_mfma_f32_16x16x32_bf16 v[48:51], v[92:95], v[116:119], v[48:51]\n\t" \
  "v_mfma_f32_16x16x32_bf16 v[52:55], v[92:95], v[120:123], v[52:55]\n\tv_mfma_f32_16x16x32_bf16 v[56:59], v[92:95], v[124:127], v[56:59]\n\t" \
  "v_mfma_f32_16x16x32_bf16 v[60:63], v[92:95], v[128:131], v[60:63]\n\ts_waitcnt lgkmcnt(0)\n\t" \
  "v_mfma_f32_16x16x32_bf16 v[64:67], v[96:99], v[116:119], v[64:67]\n\tv_mfma_f32_16x16x32_bf16 v[68:71], v[96:99], v[120:123], v[68:71]\n\t" \
  "v_mfma_f32_16x16x32_bf16 v[72:75], v[96:99], v[124:127], v[72:75]\n\tv_mfma_f32_16x16x32_bf16 v[76:79], v[96:99], v[128:131], v[76:79]\n\ts_nop 7\n\t" \
  "ds_write_b128 v161, v[0:3] offset:0\n\tds_write_b128 v161, v[16:19] offset:1024\n\tds_write_b128 v161, v[32:35] offset:2048\n\t" \
  "ds_write_b128 v161, v[48:51] offset:3072\n\tds_write_b128 v161, v[64:67] offset:4096\n\tds_write_b128 v161, v[4:7] offset:5120\n\t" \
  "ds_write_b128 v161, v[20:23] offset:6144\n\tds_write_b128 v161, v[36:39] offset:7168\n\tds_write_b128 v161, v[52:55] offset:8192\n\t" \
  "ds_write_b128 v161, v[68:71] offset:9216\n\tds_write_b128 v161, v[8:11] offset:10240\n\tds_write_b128 v161, v[24:27] offset:11264\n\t" \
  "ds_write_b128 v161, v[40:43] offset:12288\n\tds_write_b128 v161, v[56:59] offset:13312\n\tds_write_b128 v161, v[72:75] offset:14336\n\t" \
  "ds_write_b128 v161, v[12:15] offset:15360\n\tds_write_b128 v161, v[28:31] offset:16384\n\tds_write_b128 v161, v[44:47] offset:17408\n\t" \
  "ds_write_b128 v161, v[60:63] offset:18432\n\tds_write_b128 v161, v[76:79] offset:19456\n\ts_branch .Lde%=\n\t.Lfa%=:\n\ts_mov_b32 %[t1], 0x100000\n\t" \
  ".Lga%=:\n\tds_read_b32 v164, v166\n\ts_waitcnt lgkmcnt(0)\n\tv_cmp_gt_u32 vcc, 8, v164\n\ts_cbranch_vccz .Lha%=\n\ts_sleep 2\n\t" \
  "s_sub_u32 %[t1], %[t1], 1\n\ts_cbranch_scc0 .Lga%=\n\ts_branch .Lha%=\n\t.Lfb%=:\n\ts_mov_b32 %[t1], 0x100000\n\t.Lgb%=:\n\tds_read_b32 v164, v166\n\t" \
  "s_waitcnt lgkmcnt(0)\n\tv_cmp_gt_u32 vcc, 8, v164\n\ts_cbranch_vccz .Lhb%=\n\ts_sleep 2\n\ts_sub_u32 %[t1], %[t1], 1\n\ts_cbranch_scc0 .Lgb%=\n\t" \
  "s_branch .Lhb%=\n\t.Lfc%=:\n\ts_mov_b32 %[t1], 0x100000\n\t.Lgc%=:\n\tds_read_b32 v164, v166\n\ts_waitcnt lgkmcnt(0)\n\tv_cmp_gt_u32 vcc, 8, v164\n\t" \
  "s_cbranch_vccz .Lhc%=\n\ts_sleep 2\n\ts_sub_u32 %[t1], %[t1], 1\n\ts_cbranch_scc0 .Lgc%=\n\ts_branch .Lhc%=\n\t.Lfd%=:\n\t" \
  "s_mov_b32 %[t1], 0x100000\n\t.Lgd%=:\n\tds_read_b32 v164, v166\n\ts_waitcnt lgkmcnt(0)\n\tv_cmp_gt_u32 vcc, 8, v164\n\ts_cbranch_vccz .Lhd%=\n\t" \
  "s_sleep 2\n\ts_sub_u32 %[t1], %[t1], 1\n\ts_cbranch_scc0 .Lgd%=\n\ts_branch .Lhd%=\n\t.Lde%=:\n\t"
#define DN_CLOB "v0","v1","v2","v3","v4","v5","v6","v7","v8","v9","v10","v11","v12","v13","v14","v15","v16","v17","v18","v19","v20","v21","v22","v23","v24","v25","v26","v27","v28","v29","v30","v31","v32","v33","v34","v35","v36","v37","v38","v39","v40","v41","v42","v43","v44","v45","v46","v47","v48","v49","v50","v51","v52","v53","v54","v55","v56","v57","v58","v59","v60","v61","v62","v63","v64","v65","v66","v67","v68","v69","v70","v71","v72","v73","v74","v75","v76","v77","v78","v79","v80","v81","v82","v83","v84","v85","v86","v87","v88","v89","v90","v91","v92","v93","v94","v95","v96","v97","v98","v99","v100","v101","v102","v103","v104","v105","v106","v107","v108","v109","v110","v111","v112","v113","v114","v115","v116","v117","v118","v119","v120","v121","v122","v123","v124","v125","v126","v127","v128","v129","v130","v131","v132","v133","v134","v135","v136","v137","v138","v139","v140","v141","v142","v143","v144","v145","v146","v147","v152","v153","v154","v155","v156","v157","v158","v159"
__device__ __forceinline__ void q_offsets(int wave, int lane, unsigned& vx, unsigned& vw0, unsigned& vw1) {
  const int t = 8 * wave + (lane >> 3);
  vx = t * 1280 + (((lane & 7) ^ ((t >> 1) & 7)) << 4);
  int e = 64 * wave + lane, rho = e / 10, cc = e - rho * 10;
  vw0 = ((rho & 48) + ((rho & 4) << 1) + ((rho & 8) >> 1) + (rho & 3)) * 1280 + cc * 16;
  e = 512 + 32 * (wave & 3) + (lane & 31); rho = e / 10; cc = e - rho * 10;
  vw1 = ((rho & 48) + ((rho & 4) << 1) + ((rho & 8) >> 1) + (rho & 3)) * 1280 + cc * 16;
}
template <int UP>
__device__ __forceinline__ void q_proj(const Params& p, lptr lds, const int wave, const int lane, const int ti, const int tj, WT& wtmp,
                                       const char* (&ts)[4]) {
  const unsigned lds0 = (unsigned)(uintptr_t)lds;
  const char* xsrc = p.x + (size_t)ti * 64 * 1280;
  const char* wsrc = p.wq + tj * 160;
  unsigned vx, vw0, vw1;
  q_offsets(wave, lane, vx, vw0, vw1);
  const int qrot = (((ti & 7) + 3 * (tj & 3)) % 10);
  int pi = qrot, pr = qrot;
  auto issue = [&](int d) {
#pragma unroll
    for (int k = 0; k < 2; ++k) {
      const unsigned dst = lds0 + (d == 2 ? L_KV : L_Q + (d % 3) * 2 * Q_STAGE) + k * Q_STAGE;
      dma16(vx, xsrc + pi * 128, dst + wave * 1024);
      dma16(vw0, wsrc + pi * 81920, dst + 8192 + wave * 1024);
      if (UP) dma16_half(vw1, wsrc + pi * 81920, dst + 16384 + (wave & 3) * 512);
      pi = pi == 9 ? 0 : pi + 1;
    }
  };
  pi = pi + 4 >= 10 ? pi - 6 : pi + 4;
  __builtin_amdgcn_s_setprio(2);
  const int t4 = wave & 3, g = lane >> 4, i16 = lane & 15, q4 = i16 >> 2, pp = lane & 3;
  const int tok = 16 * t4 + i16;
  const int ox0 = tok * 128 + ((g ^ ((tok >> 1) & 7)) << 4), ox1 = ox0 ^ 64;
  const int ow = 8192 + (16 * (g >> 1) + 4 * (g & 1) + q4) * 160 + UP * 96 + pp * 8;
  lptr bg = lds + L_GW + g * 16;
  f32x4 qc[3] = {};
  bf16x8 xf[4], wf[4][3];
  auto rd = [&](int d) {
    lptr sb = lds + (d == 2 ? L_KV : L_Q + (d % 3) * 2 * Q_STAGE);
#pragma unroll
    for (int k = 0; k < 4; ++k) {
      xf[k] = ld128(sb + ((k & 1) ? ox1 : ox0) + (k >> 1) * Q_STAGE);
      lptr wp = sb + ow + (k >> 1) * Q_STAGE + (k & 1) * 5120;
      wf[k][0] = ldtr2(wp, 1280);
      wf[k][1] = ldtr2(wp + 32, 1280);
      wf[k][2] = UP ? ld128(bg + pr * 128 + (k & 1) * 64) : ldtr2(wp + 64, 1280);
      if (k & 1) pr = pr == 9 ? 0 : pr + 1;
    }
    SBAR();
  };
  constexpr int NP = 2 * (2 + UP);
  vmcnt<NP>();
  arrive(lds, C_Q);
  issue(2);
  if (UP) wait_ge(lds, C_GW, 4);
#define QSTEP(d)                                                                         \
  do {                                                                                   \
    wait_ge(lds, C_Q, 8 * (d + 1));      \
    if (d == 1 || d == 2) issue(d + 2);                                                  \
    rd(d);                                                                               \
    if (d < 4) { vmcnt<(d < 3 ? NP : 0)>(); arrive(lds, C_Q); } \
    else arrive(lds, C_QDONE);                                                           \
    _Pragma("unroll") for (int k = 0; k < 4; ++k)                                        \
      _Pragma("unroll") for (int b = 0; b < 3; ++b) qc[b] = MFMA16(wf[k][b], xf[k], qc[b]); \
    PIN(qc[0]); PIN(qc[1]); PIN(qc[2]); SBAR();                                          \
  } while (0)
  QSTEP(0); QSTEP(1); QSTEP(2);
#pragma unroll
  for (int i = 0; i < 4; ++i) {
    ts[i] = p.wd;
    if (UP && wave >= 5) {
      const int rem = min(ti * 3 + wave - 5 + i * 96, 339) * 64 + lane, j = tj, R = rem / 10;
      const int Q = R / 544, t = R - Q * 544, w = t - 384, rho = w & 31;
      const int quad = (((rho & 8) ? 0x83972 : 0x61450) >> (4 * (w >> 5))) & 15;
      const int row = 160 * Q + 40 * (2 * (rho >> 4) + ((rho >> 2) & 1)) + 4 * quad + (rho & 3);
      ts[i] = (t < 384 ? p.wd + (384 * Q + t) * 1280 : p.wo + row * 1280) + j * 160 + (rem - R * 10) * 16;
      PIN(ts[i]);
    }
  }
  QSTEP(3); QSTEP(4);
  constexpr float QS = 0.22360679774997896f * 1.4426950408889634f;
  lptr qi = lds + L_QI + tok * 160 + UP * 96 + g * 8;
#pragma unroll
  for (int b = 0; b < 3 - UP; ++b) *(LAS u32x2*)(qi + b * 32) = (u32x2){cvtpk(qc[b][0] * QS, qc[b][1] * QS), cvtpk(qc[b][2] * QS, qc[b][3] * QS)};
  if (UP) {
    const float lg = bflo(cvtpk(qc[2][0], 0.f));
    float gt = __builtin_amdgcn_rcpf(1.f + __builtin_amdgcn_exp2f(-1.4426950408889634f * lg));
    gt = bflo(cvtpk(gt, 0.f));
    *(LAS float*)(lds + L_GATE + tok * 8 + (g & 1) * 4) = (g & 1) ? bflo(cvtpk(1.f - gt, 0.f)) : gt;
  }
  arrive(lds, C_QI);
}

__device__ __forceinline__ void role_a(const Params& p, lptr lds, const int wave, const int lane, const int ti, const int tj) {
  const int tb = wave & 1, hp = (wave >> 1) & 1, kh = wave >> 2;
  const int r32 = lane & 31, hh = lane >> 5;
  const unsigned lds0 = (unsigned)(uintptr_t)lds;
  WT wtmp = {0u, 0u};
  const char* ts[4];
  if (wave < 4) q_proj<0>(p, lds, wave, lane, ti, tj, wtmp, ts);
  else q_proj<1>(p, lds, wave, lane, ti, tj, wtmp, ts);
  const int pc0 = ti * 3 + wave - 5;
  char* wt0 = p.ws + WS_WT + tj * 348160 + pc0 * 1024;
  __builtin_amdgcn_s_setprio(3);
  f32x16 oa = {}, ob = {};
  u32x4 z0 = {}, z1 = {}, z2 = {}, z3 = {};
  PIN(oa); PIN(ob); PIN(z0); PIN(z1); PIN(z2); PIN(z3);
  wait_ge(lds, C_KVRDY, 4);
  const u32x2 tk = *(volatile LAS u32x2*)(lds + L_CTRL + 4 * C_TAG);
  wait_ge(lds, C_QI, 8);
  u32x4 qa0, qb0;
  u32x2 qa1, qb1;
  float na, nb;
  {
    lptr qi = lds + L_QI + (32 * tb + r32) * 160 + hp * 80;
    const u32x2 a0 = *(LAS u32x2*)(qi + hh * 8), a1 = *(LAS u32x2*)(qi + 16 + hh * 8);
    const u32x2 b0 = *(LAS u32x2*)(qi + 40 + hh * 8), b1 = *(LAS u32x2*)(qi + 56 + hh * 8);
    u32x2 a2 = *(LAS u32x2*)(qi + 32), b2 = *(LAS u32x2*)(qi + 72);
    if (hh) { a2 = u32x2{}; b2 = u32x2{}; }
    qa0 = (u32x4){a0[0], a0[1], a1[0], a1[1]}; qa1 = a2;
    qb0 = (u32x4){b0[0], b0[1], b1[0], b1[1]}; qb1 = b2;
    float n[4] = {0.f, 0.f, 0.f, 0.f};
#pragma unroll
    for (int e = 0; e < 6; ++e) {
      const unsigned ua = e < 4 ? qa0[e] : qa1[e - 4], ub = e < 4 ? qb0[e] : qb1[e - 4];
      n[e & 1] = __builtin_amdgcn_fdot2_f32_bf16(__builtin_bit_cast(bf16x2_t, ua), __builtin_bit_cast(bf16x2_t, ua), n[e & 1], false);
      n[2 + (e & 1)] = __builtin_amdgcn_fdot2_f32_bf16(__builtin_bit_cast(bf16x2_t, ub), __builtin_bit_cast(bf16x2_t, ub), n[2 + (e & 1)], false);
    }
    na = swap_add(n[0] + n[1]); nb = swap_add(n[2] + n[3]);
  }
  const unsigned tag = __builtin_amdgcn_readfirstlane(tk[0]);
  const float kmax2 = __uint_as_float(tk[1]) * 1.02f;
  const float ba = __builtin_amdgcn_sqrtf(na * kmax2), bb = __builtin_amdgcn_sqrtf(nb * kmax2);
  const bool fast = __all(ba <= 40.f && bb <= 40.f);
  __builtin_amdgcn_s_setprio(2);

  const char* kvsrc = p.ws + WS_KV + wave * 2048;
  const unsigned vlin = lane * 16;
  const unsigned k0b = lds0 + L_KV + kh * 2048 + lane * 16, k1b = lds0 + L_KV + 9216 + kh * 512 + r32 * 8;
  const unsigned vb = lds0 + (r32 < 20 ? L_KV + 4096 + kh * 2560 + (20 * hh + r32) * 16 : r32 == 20 ? L_ONES : L_ONES + 16);
  unsigned vstep = r32 < 20 ? KV_CHUNK : 0u;
  auto kv_issue = [&](int c) {
    if (wave < 5) {
      dma16_dev(vlin, kvsrc + c * KV_CHUNK, lds0 + L_KV + (c & 3) * KV_CHUNK + wave * 2048);
      dma16_dev(vlin, kvsrc + c * KV_CHUNK + 1024, lds0 + L_KV + (c & 3) * KV_CHUNK + wave * 2048 + 1024);
    }
  };
  float ma = 0.f, mb = 0.f;
#define EX(v) __builtin_amdgcn_exp2f(v)
#define BF(P) __builtin_bit_cast(bf16x8, P)
  if (fast) {
    const char* src = kvsrc + 2 * KV_CHUNK;
    unsigned it, tgt, sv, cnt;
    asm volatile(ATTN_LOOP
                 : "+{v[0:15]}"(oa), "+{v[16:31]}"(ob), "+{v[84:87]}"(z0), "+{v[88:91]}"(z1), "+{v[100:103]}"(z2), "+{v[104:107]}"(z3), "+{s[40:41]}"(src), "+{v145}"(vstep), "+{v149}"(wtmp.a), "+{v[150:151]}"(ts[0]), "+{v[152:153]}"(ts[1]), "+{v[154:155]}"(ts[2]),
                   "+{v[156:157]}"(ts[3]), [it] "=&s"(it), [tgt] "=&s"(tgt),
                   [sv] "=&s"(sv), [cnt] "=&s"(cnt)
                 : "{v[64:67]}"(qa0), "{v[68:69]}"(qa1), "{v[70:73]}"(qb0), "{v[74:75]}"(qb1), "{v108}"(-ba), "{v124}"(-bb),
                   "{v140}"(k0b), "{v141}"(k1b), "{v142}"(vb), "{v146}"(vlin), "{v147}"(lds0 + L_CTRL + 4 * C_KV), "{v148}"(1u),
                   "{v82}"((tj * 32 + ti) * 128), "{v83}"(tag), [tf] "s"(p.ws + WS_TF), [pc] "s"(pc0),
                   [w0] "s"(wt0), [w1] "s"(wt0 + 96 * 1024), [w2] "s"(wt0 + 192 * 1024), [w3] "s"(wt0 + 288 * 1024), [dst] "s"(lds0 + L_KV + wave * 2048), [dma] "s"(wave)
                 : ATTN_CLOB, "scc", "memory");
  } else {
    typedef __attribute__((ext_vector_type(4))) short bf16x4;
    const f32x16 zero16 = {};
    ma = -INFINITY; mb = -INFINITY;
    kv_issue(2);
    if (wave >= 5) {
#pragma unroll
      for (int i = 0; i < 4; ++i)
        if (i < 3 || pc0 < 52) st16_dev(wt0 + i * 96 * 1024, lane * 16, *(const GAS u32x4*)ts[i]);
    }
#pragma unroll 1
    for (int c = 0; c < 16; ++c) {
      const int so = (c & 3) * KV_CHUNK;
      const unsigned va = vb + (c & 3) * vstep;
#pragma unroll 1
      for (int t = 0; t < 2; ++t) {
        const bf16x8 k0 = ld128((lptr)(uintptr_t)(k0b + so + t * 1024));
        const bf16x4 k1 = *(LAS bf16x4*)(uintptr_t)(k1b + so + t * 256);
        const bf16x8 v0 = ld128((lptr)(uintptr_t)(va + t * 1280)), v1 = ld128((lptr)(uintptr_t)(va + t * 1280 + 640));
#pragma unroll
        for (int w = 0; w < 2; ++w) {
          f32x16 s = MFMA32(k0, BF(w ? qb0 : qa0), zero16);
          s = __builtin_amdgcn_mfma_f32_32x32x8bf16_1k(k1, __builtin_bit_cast(bf16x4, w ? qb1 : qa1), s, 0, 0, 0);
          float m = s[0];
#pragma unroll
          for (int r = 1; r < 16; ++r) m = __builtin_fmaxf(m, s[r]);
          const float mo = w ? mb : ma;
          m = __builtin_fmaxf(swap_max(m), mo);
          const float al = EX(mo - m);
          u32x4 p0, p1;
#pragma unroll
          for (int r = 0; r < 4; ++r) { p0[r] = cvtpk(EX(s[2 * r] - m), EX(s[2 * r + 1] - m)); p1[r] = cvtpk(EX(s[8 + 2 * r] - m), EX(s[9 + 2 * r] - m)); }
          if (w) { ob *= al; ob = MFMA32(v0, BF(p0), ob); ob = MFMA32(v1, BF(p1), ob); mb = m; }
          else   { oa *= al; oa = MFMA32(v0, BF(p0), oa); oa = MFMA32(v1, BF(p1), oa); ma = m; }
        }
        if (t == 0) {
          if (c < 14 && wave < 5) VMCNT(2); else VMCNT(0);
          arrive(lds, C_KV);
          wait_ge(lds, C_KV, 8 * (c + 1));
          if (c == 6 && wave == 7 && lane == 0) st4_dev(p.ws + WS_TF, (tj * 32 + ti) * 128, tag);
          if (c + 3 < 16) kv_issue(c + 3);
        }
      }
    }
  }
  __builtin_amdgcn_s_setprio(3);
  {
    int lane_e;
    asm volatile("v_mbcnt_lo_u32_b32 %0, -1, 0\n\tv_mbcnt_hi_u32_b32 %0, -1, %0" : "=v"(lane_e));
    const int lane = lane_e, r32 = lane & 31, hh = lane >> 5;
    const f32x16 snd = kh ? oa : ob;
    lptr xw = lds + L_KV + wave * 3328;
    *(LAS f32x4*)(xw + lane * 16) = (f32x4){snd[0], snd[1], snd[2], snd[3]};
    *(LAS f32x4*)(xw + 1024 + lane * 16) = (f32x4){snd[4], snd[5], snd[6], snd[7]};
    *(LAS f32x4*)(xw + 2048 + lane * 16) = (f32x4){snd[8], snd[9], snd[10], snd[11]};
    *(LAS float*)(xw + 3072 + lane * 4) = kh ? ma : mb;
    arrive(lds, C_P + (wave & 3));
    arrive(lds, C_X);
    wait_ge(lds, C_P + (wave & 3), 2);
    lptr xr = lds + L_KV + (wave ^ 4) * 3328;
    const f32x4 r0 = *(LAS f32x4*)(xr + lane * 16), r1 = *(LAS f32x4*)(xr + 1024 + lane * 16), r2 = *(LAS f32x4*)(xr + 2048 + lane * 16);
    const float mr = *(LAS float*)(xr + 3072 + lane * 4);
    const f32x16 own = kh ? ob : oa;
    const float mo = kh ? mb : ma, mm = __builtin_fmaxf(mo, mr);
    const float fo = EX(mo - mm), fr = EX(mr - mm);
    float o[12];
#pragma unroll
    for (int r = 0; r < 4; ++r) { o[r] = own[r] * fo + r0[r] * fr; o[4 + r] = own[4 + r] * fo + r1[r] * fr; o[8 + r] = own[8 + r] * fo + r2[r] * fr; }
    const float gate = *(LAS float*)(lds + L_GATE + (32 * tb + r32) * 8);
    const float sc = gate * __builtin_amdgcn_rcpf(upper_half(o[8]));
    const int P = 2 * tj + hp;
    const int t16 = 4 * ti + 2 * tb + (r32 >> 4);
    char* ob_ = p.ws + WS_O;
    const unsigned base = (t16 * 80 + 20 * (P >> 2) + (P & 3)) * 256 + (r32 & 15) * 16;
    st16_dev(ob_, base + (kh + 3 * hh) * 1024,
             (u32x4){cvtpk(o[0] * sc, o[1] * sc), cvtpk(o[2] * sc, o[3] * sc), cvtpk(o[4] * sc, o[5] * sc), cvtpk(o[6] * sc, o[7] * sc)});
    if (hh == 0) st8_dev(ob_, base + 2048 + kh * 8, (u32x2){cvtpk(o[8] * sc, o[9] * sc), cvtpk(o[10] * sc, o[11] * sc)});
    VMCNT(0);
    if (lane == 0) st4_dev(p.ws + WS_OF, ((ti * 8 + tj) * 8 + wave) * 128, tag);
  }
  asm volatile("" ::"v"(wtmp.a), "v"(wtmp.b));
}

#define LANE_IDS(sfx)                                                                           \
  int lane##sfx;                                                                                \
  asm volatile("v_mbcnt_lo_u32_b32 %0, -1, 0\n\tv_mbcnt_hi_u32_b32 %0, -1, %0" : "=v"(lane##sfx));   \
  const int lane = lane##sfx, r32 = lane & 31, hh = lane >> 5, g4 = lane >> 4, i16 = lane & 15, q4 = i16 >> 2, pp = lane & 3; \
  (void)r32; (void)hh; (void)g4; (void)i16; (void)q4; (void)pp
__device__ __forceinline__ void role_g(const Params& p, lptr lds, const int wave, const int ti, const int tj,
                                       const int fI, const int fc, const unsigned tag) {
  const int gw = wave - 8;
  const unsigned lds0 = (unsigned)(uintptr_t)lds;
  WT wtmp = {0u, 0u};
  const int t16 = 4 * ti + (tj >> 1), kind = tj & 1;
  {
    LANE_IDS(0);
    __builtin_amdgcn_s_setprio(3);
    bf16x8 xf[5];
    if (gw == 0 && lane == 0) *(LAS unsigned*)(lds + L_CTRL + 4 * C_TAG) = tag;
    asm volatile("" ::"s"(tag));
    asm volatile("s_waitcnt vmcnt(0)" : "={v[140:143]}"(xf[0]), "={v[144:147]}"(xf[1]), "={v[148:151]}"(xf[2]), "={v[152:155]}"(xf[3]), "={v[156:159]}"(xf[4])::"memory");
    arrive(lds, C_GW);
    f32x4 d0 = {}, d1 = {};
    lptr bw = lds + L_G + gw * 7168 + (8 * g4 + q4) * 40;
#pragma unroll
    for (int st = 0; st < 5; ++st) {
      bf16x8 w0 = ldtr2(bw + st * 1280 + pp * 8, 160), w1 = ldtr2(bw + st * 1280 + 32, 160);
      if (kind) { d0 = MFMA16(xf[st], w0, d0); d1 = MFMA16(xf[st], w1, d1); }
      else      { d0 = MFMA16(w0, xf[st], d0); d1 = MFMA16(w1, xf[st], d1); }
    }
    lptr sc = lds + L_G + 28672;
    if (gw != 0) {
      *(LAS f32x4*)(sc + gw * 2048 + lane * 16) = d0;
      *(LAS f32x4*)(sc + gw * 2048 + 1024 + lane * 16) = d1;
      arrive(lds, C_G);
    } else {
      wait_ge(lds, C_G, 3);
#pragma unroll
      for (int w = 1; w < 4; ++w) { d0 += *(LAS f32x4*)(sc + w * 2048 + lane * 16); d1 += *(LAS f32x4*)(sc + w * 2048 + 1024 + lane * 16); }
      arrive(lds, C_G);
      const u32x2 e0 = {cvtpk(d0[0], d0[1]), cvtpk(d0[2], d0[3])}, e1 = {cvtpk(d1[0], d1[1]), cvtpk(d1[2], d1[3])};
      const int key0 = 16 * t16, u = 2 * (key0 >> 10) + ((key0 >> 5) & 1), s16 = (key0 >> 4) & 1;
      char* cb = p.ws + WS_KV + ((key0 & 1023) >> 6) * KV_CHUNK;
      float n2 = 0.f;
      if (kind) {
        const unsigned o = 4096 + (2 * u + s16) * 640 + (g4 & 1) * 320 + (g4 >> 1) * 8;
        st8_dev(cb, o + i16 * 16, e0);
        if (i16 < 4) st8_dev(cb, o + (16 + i16) * 16, e1);
      } else {
        const int key = 16 * s16 + i16;
        st8_dev(cb, u * 1024 + ((g4 & 1) * 32 + key) * 16 + (g4 >> 1) * 8, e0);
        if (g4 == 0) st8_dev(cb, 9216 + u * 256 + key * 8, e1);
        n2 = bflo(e0[0]) * bflo(e0[0]) + bfhi(e0[0]) * bfhi(e0[0]) + bflo(e0[1]) * bflo(e0[1]) + bfhi(e0[1]) * bfhi(e0[1]);
        if (g4 == 0) n2 += bflo(e1[0]) * bflo(e1[0]) + bfhi(e1[0]) * bfhi(e1[0]) + bflo(e1[1]) * bflo(e1[1]) + bfhi(e1[1]) * bfhi(e1[1]);
        n2 += __shfl_xor(n2, 16); n2 += __shfl_xor(n2, 32);
#pragma unroll
        for (int m = 1; m < 16; m <<= 1) { const float o2 = __shfl_xor(n2, m); n2 = (o2 > n2 || o2 != o2) ? o2 : n2; }
      }
      VMCNT(0);
      if (lane == 0) st8_dev(p.ws + WS_KVF, (ti * 8 + tj) * 8, (u32x2){tag, __float_as_uint(n2)});
    }
  }
  __builtin_amdgcn_s_setprio(3);
  unsigned gs = 4;
  const int tm = gw & 1, fh = gw >> 1;
  f32x16 acc[2][3];
  bool tf_ok;
  {
    LANE_IDS(1);
    const char* xsrc = p.x + (size_t)(fI * 128 + gw * 32) * 1280;
    const char* gsrc = p.wg + (size_t)gw * 16 * 3072 + fc * 192;
    const char* usrc = p.wu + (size_t)gw * 16 * 3072 + fc * 192;
    u32x4 vx, bx;
    unsigned vw[3];
    const int tok = 64 * tm + r32;
#pragma unroll
    for (int m = 0; m < 4; ++m) {
      const int t = 8 * m + (lane >> 3);
      vx[m] = t * 1280 + (((lane & 7) ^ ((t >> 1) & 7)) << 4) - m * 1024;
      bx[m] = lds0 + tok * 128 + (((hh ^ ((tok >> 1) & 7)) << 4) ^ (m * 32));
    }
#pragma unroll
    for (int q = 0; q < 3; ++q) { const int e = 64 * q + lane, rr = e / 12, cc = e - rr * 12; vw[q] = rr * 3072 + cc * 16 - q * 1024; }
    const unsigned bw = lds0 + (g4 & 1) * 12320 + (8 * (g4 >> 1) + q4) * 192 + fh * 96 + pp * 8;
    const unsigned xd = lds0 + gw * 4096, wd = lds0 + gw * 3072;
    auto x_req = [&](int xo) {
      dma16(vx[0], xsrc, xd + xo); dma16n<1024>(vx[1], xsrc); dma16n<2048>(vx[2], xsrc); dma16n<3072>(vx[3], xsrc);
      xsrc += 128;
    };
    auto w_req = [&](int wo_) {
      dma16(vw[0], gsrc, wd + wo_); dma16n<1024>(vw[1], gsrc); dma16n<2048>(vw[2], gsrc);
      dma16(vw[0], usrc, wd + wo_ + 12320); dma16n<1024>(vw[1], usrc); dma16n<2048>(vw[2], usrc);
      gsrc += 64 * 3072; usrc += 64 * 3072;
    };
#pragma unroll
    for (int a = 0; a < 6; ++a) { acc[a / 3][a % 3] = f32x16{}; PIN(acc[a / 3][a % 3]); }
    wait_ge(lds, C_G, 4);
    if (gw == 1) {
      float km = 0.f;
      wait_ge(lds, C_Q, 24);
#pragma unroll 1
      for (unsigned s = 0; s < SPIN_GLB; ++s) {
        u32x2 g0, g1, g2, g3;
        asm volatile("global_load_dwordx2 %0, %4, %5 sc1\n\tglobal_load_dwordx2 %1, %4, %5 offset:%6 sc1\n\t"
                     "global_load_dwordx2 %2, %4, %5 offset:%7 sc1\n\tglobal_load_dwordx2 %3, %4, %5 offset:%8 sc1\n\ts_waitcnt vmcnt(0)"
                     : "=&v"(g0), "=&v"(g1), "=&v"(g2), "=&v"(g3) : "v"(lane * 32), "s"(p.ws + WS_KVF), "i"(8), "i"(16), "i"(24) : "memory");
        const u32x4 f0 = {g0[0], g0[1], g1[0], g1[1]}, f1 = {g2[0], g2[1], g3[0], g3[1]};
        const bool ok = f0[0] == tag && f0[2] == tag && f1[0] == tag && f1[2] == tag;
        if (__all(ok)) {
          const float a = __uint_as_float(f0[1]), b = __uint_as_float(f0[3]), c = __uint_as_float(f1[1]), d = __uint_as_float(f1[3]);
          km = __builtin_fmaxf(__builtin_fmaxf(a, b), __builtin_fmaxf(c, d));
          if (a != a || b != b || c != c || d != d) km = INFINITY;
          break;
        }
        __builtin_amdgcn_s_sleep(6);
      }
      arrive(lds, C_SEEN);
#pragma unroll
      for (int m = 1; m < 64; m <<= 1) km = __builtin_fmaxf(km, __shfl_xor(km, m));
      if (lane == 0) *(LAS float*)(lds + L_CTRL + 4 * C_KMAX) = km;
    }
    else wait_ge(lds, C_SEEN, 1);
    wait_ge(lds, C_Q, 32);
    const char* kvs = p.ws + WS_KV + gw * 5120;
    dma16_dev(lane * 16, kvs, lds0 + L_KV + gw * 5120); dma16n_dev<1024>(lane * 16, kvs); dma16n_dev<2048>(lane * 16, kvs); dma16n_dev<3072>(lane * 16, kvs);
    dma16_dev(lane * 16, kvs + 4096, lds0 + L_KV + gw * 5120 + 4096);
    x_req(UPX0); w_req(UPW0);
    VMCNT(10);
    arrive(lds, C_KVRDY);
    wait_ge(lds, C_QDONE, 8);
    w_req(UPW1); x_req(UPX1);
    VMCNT(10);
    arrive(lds, C_G);
    wait_ge(lds, C_G, 8);
    unsigned t0, t1, tgt = 8, b0 = bw + UPW0, b1 = bw + UPW1, b2 = bw + UPW2, w0 = wd + UPW0, w1 = wd + UPW1, w2 = wd + UPW2;
    unsigned tfv;
    asm volatile(UP_LOOP
                 : "+{v[0:15]}"(acc[0][0]), "+{v[16:31]}"(acc[1][0]), "+{v[32:47]}"(acc[0][1]), "+{v[48:63]}"(acc[1][1]), "+{v[64:79]}"(acc[0][2]),
                   "+{v[80:95]}"(acc[1][2]), "+{v140}"(b0), "+{v141}"(b1), "+{v142}"(b2), [w0] "+s"(w0), [w1] "+s"(w1), [w2] "+s"(w2), [tgt] "+s"(tgt), "={v154}"(tfv), "+{s[42:43]}"(gsrc), "+{s[44:45]}"(usrc), "+{s[46:47]}"(xsrc), [t0] "=&s"(t0), [t1] "=&s"(t1)
                 : "{v[136:139]}"(bx), "{v[144:147]}"(vx), "{v159}"(vw[0]), "{v160}"(vw[1]),
                   "{v161}"(vw[2]), "{v150}"(lds0 + L_CTRL + 4 * C_G), "{v151}"(1u), "{v153}"(lds0 + (lane ? L_DUMMY + lane * 4 : L_CTRL + 4 * C_G)),
                   "{v158}"((tj * 32 + r32) * 128), [tf] "s"(p.ws + WS_TF), [xd] "s"(xd)
                 : UP_CLOB, "scc", "memory");
    gs = 48;
    tf_ok = __all(tfv == tag);
  }
  {
    LANE_IDS(2);
    __builtin_amdgcn_s_setprio(3);
    constexpr float C0 = -2.f * 0.7978845608028654f * 1.4426950408889634f, C1 = C0 * 0.044715f;
    char* hb = p.ws + WS_H;
#pragma unroll
    for (int a = 0; a < 2; ++a)
#pragma unroll
      for (int f = 0; f < 3; ++f) {
        typedef __attribute__((ext_vector_type(2))) float f32x2;
        u32x4 hp;
#pragma unroll
        for (int r = 0; r < 4; ++r) {
          const f32x2 g = {acc[a][f][2 * r], acc[a][f][2 * r + 1]}, u = {acc[a][f][2 * r + 8], acc[a][f][2 * r + 9]};
          const f32x2 z = g * __builtin_elementwise_fma(g * g, (f32x2){C1, C1}, (f32x2){C0, C0});
          const f32x2 d = (f32x2){__builtin_amdgcn_exp2f(z[0]), __builtin_amdgcn_exp2f(z[1])} + (f32x2){1.f, 1.f};
          const f32x2 h = (g * u) * (f32x2){__builtin_amdgcn_rcpf(d[0]), __builtin_amdgcn_rcpf(d[1])};
          hp[r] = cvtpk(h[0], h[1]);
        }
        const unsigned off = ((fI * 8 + tm * 4 + a * 2 + (r32 >> 4)) * 192 + fc * 12 + (3 * fh + f) * 2 + hh) * 256 + i16 * 16;
        st16_dev(hb, off, hp);
      }
    __builtin_amdgcn_s_setprio(3);
    VMCNT(0);
    if (lane == 0) st4_dev(p.ws + WS_HF, (((fI * 2 + tm) * 16 + fc) * 2 + fh) * 128, tag);
  }
  LANE_IDS(3);
  const char* dwsrc = p.ws + WS_WT + tj * 348160 + gw * (544 * 160);
  const char* dhsrc = p.ws + WS_H + (size_t)(4 * ti * 192 + gw * 48) * 256;
  const unsigned ring = lds0 + L_G + gw * DN_WAVE;
  const unsigned vl = lane * 16;
  {
    wait_ge(lds, C_G, gs);
    if (!tf_ok) {
#pragma unroll 1
      for (unsigned s = 0; s < SPIN_GLB; ++s) {
        unsigned f0;
        asm volatile("global_load_dword %0, %1, %2 sc1\n\ts_waitcnt vmcnt(0)" : "=&v"(f0) : "v"((tj * 32 + r32) * 128), "s"(p.ws + WS_TF) : "memory");
        if (__all(f0 == tag)) break;
        __builtin_amdgcn_s_sleep(8);
      }
    }
#pragma unroll
    for (int q = 0; q < 25; q += 4) {
      dma16_dev(vl, dwsrc + q * 1024, ring + q * 1024);
      if (q < 24) { dma16n_dev<1024>(vl, dwsrc + q * 1024); dma16n_dev<2048>(vl, dwsrc + q * 1024); dma16n_dev<3072>(vl, dwsrc + q * 1024); }
    }
#pragma unroll 1
    for (unsigned s = 0; s < SPIN_GLB; ++s) {
      unsigned f;
      asm volatile("global_load_dword %0, %1, %2 sc1\n\ts_waitcnt vmcnt(0)" : "=&v"(f) : "v"((ti * 32 + gw * 8 + (lane & 7)) * 128), "s"(p.ws + WS_HF) : "memory");
      if (__all(f == tag)) break;
      __builtin_amdgcn_s_sleep(8);
    }
    {
      const char* wsrc = dwsrc + 5 * 5120;
      const char* hsrc = dhsrc;
      const char* ob = p.ws + WS_O + ((4 * ti) * 80 + 20 * gw) * 256 + 2048;
      unsigned t0, t1, ro, bw = ring + (16 * (g4 >> 1) + 4 * (g4 & 1) + q4) * 160 + pp * 8;
      asm volatile(DN_LOOP
                   : "+{s[42:43]}"(wsrc), "+{s[44:45]}"(hsrc), "+{v160}"(bw), "+{v164}"(wtmp.a), [t0] "=&s"(t0), [t1] "=&s"(t1), [ro] "=&s"(ro)
                   : "{v[148:151]}"((u32x4){vl, vl + 49152, vl + 2 * 49152, vl + 3 * 49152}), "{v161}"(ring + vl), "{v162}"(vl),
                     "{v163}"((ti * 64 + gw * 16 + i16) * 128), "{v165}"(lds0 + L_GATE + i16 * 8 + 4), "{v166}"(lds0 + L_CTRL + 4 * C_X),
                     [ring] "s"(ring), [of] "s"(p.ws + WS_OF), [tag] "s"(tag), [o0] "s"(ob), [o1] "s"(ob + 20480), [o2] "s"(ob + 2 * 20480),
                     [o3] "s"(ob + 3 * 20480)
                   : DN_CLOB, "vcc", "scc", "memory");
    }
    {
    LANE_IDS(4);
    arrive(lds, C_G); gs += 4;
    wait_ge(lds, C_G, gs);
    lptr sr = lds + L_G + gw * 5120 + lane * 16;
    char* op = p.out + (size_t)(64 * ti + 16 * gw) * 1280 + tj * 160;
    const unsigned oo = i16 * 1280 + g4 * 8;
#pragma unroll
    for (int b = 0; b < 5; ++b) {
      f32x4 r = *(LAS f32x4*)(sr + b * 1024);
#pragma unroll
      for (int w = 1; w < 4; ++w) r += *(LAS f32x4*)(sr + w * DN_WAVE + b * 1024);
      asm volatile("global_store_dwordx2 %0, %1, %2 offset:%3" ::"v"(oo), "v"((u32x2){cvtpk(r[0], r[1]), cvtpk(r[2], r[3])}), "s"(op), "i"(b * 32) : "memory");
    }
    if (blockIdx.x == 0 && gw == 1 && lane == 0) st4_dev(p.ws + WS_EPOCH, 0, tag);
    }
  }
  asm volatile("" ::"v"(wtmp.a), "v"(wtmp.b));
}

__global__ __launch_bounds__(768, 3) void gated_mix_fused(Params p) {
  extern __shared__ __attribute__((aligned(16))) char smem[];
  lptr lds = (lptr)smem;
  const int wave = __builtin_amdgcn_readfirstlane(threadIdx.x >> 6), lane = threadIdx.x & 63;
  const int xcd = blockIdx.x & 7, r = blockIdx.x >> 3;
  const int ti = (xcd >> 1) * 8 + (r >> 2), tj = (xcd & 1) * 4 + (r & 3);
  const int fI = (xcd >> 1) * 4 + (r >> 3), fc = (xcd & 1) * 8 + (r & 7);
  const unsigned lds0 = (unsigned)(uintptr_t)lds;
  if (wave == 0) {
    const char* pc;
    asm volatile("s_getpc_b64 %0" : "=s"(pc));
    asm volatile("s_mov_b64 exec, %2\n\tglobal_load_dword v160, %0, %1\n\ts_mov_b64 exec, -1" ::"v"((r * 8 + lane) * 128), "s"(pc), "i"(255) : "v160", "memory");
  }
  if (wave == 11) {
    *(LAS unsigned*)(lds + (lane < 32 ? L_CTRL + (lane & 15) * 4 : L_ONES + ((lane >> 3) & 3) * 640 + (lane & 7) * 4)) = (lane < 32 || (lane & 4)) ? 0u : 0x3f803f80u;
  }
  if (__builtin_expect(wave >= 8, 1)) {
    const int gw = wave - 8, t16 = 4 * ti + (tj >> 1);
    const char* xs = p.x + (size_t)(16 * t16) * 1280 + gw * 320;
    asm volatile("global_load_dwordx4 v[140:143], %0, %1\n\tglobal_load_dwordx4 v[144:147], %0, %1 offset:64\n\tglobal_load_dwordx4 v[148:151], %0, %1 offset:128\n\t"
                 "global_load_dwordx4 v[152:155], %0, %1 offset:192\n\tglobal_load_dwordx4 v[156:159], %0, %1 offset:256"
                 ::"v"((lane & 15) * 1280 + (lane >> 4) * 16), "s"(xs)
                 : "v140", "v141", "v142", "v143", "v144", "v145", "v146", "v147", "v148", "v149", "v150", "v151", "v152", "v153", "v154", "v155", "v156", "v157", "v158", "v159", "memory");
    const char* ws_ = ((tj & 1) ? p.wv : p.wk) + gw * 6400;
    const unsigned dst = lds0 + L_G + gw * 7168;
    dma16(lane * 16, ws_, dst); dma16n<1024>(lane * 16, ws_); dma16n<2048>(lane * 16, ws_); dma16n<3072>(lane * 16, ws_);
    dma16(lane * 16, ws_ + 4096, dst + 4096); dma16n<1024>(lane * 16, ws_ + 4096);
    asm volatile("s_mov_b64 exec, 0xffff\n\tglobal_load_lds_dwordx4 %0, %1 offset:2048\n\ts_mov_b64 exec, -1" ::"v"(lane * 16), "s"(ws_ + 4096) : "memory");
    if (gw == 1) {
      dma16(lane * 16, p.gw, lds0 + L_GW);
      asm volatile("s_mov_b64 exec, 0xffff\n\tglobal_load_lds_dwordx4 %0, %1 offset:1024\n\ts_mov_b64 exec, -1" ::"v"(lane * 16), "s"(p.gw) : "memory");
    }
    asm volatile("s_waitcnt lgkmcnt(0)\n\ts_barrier" ::: "memory");
  } else {
    unsigned vx, vw0, vw1;
    q_offsets(wave, lane, vx, vw0, vw1);
    const char* xsrc = p.x + (size_t)ti * 64 * 1280;
    const char* wsrc = p.wq + tj * 160;
    int pi = (((ti & 7) + 3 * (tj & 3)) % 10);
#pragma unroll
    for (int k = 0; k < 4; ++k) {
      if (k == 1) asm volatile("s_barrier" ::"v"(vx), "v"(vw0), "v"(vw1) : "memory");
      const unsigned dst = lds0 + L_Q + k * Q_STAGE;
      dma16(vx, xsrc + pi * 128, dst + wave * 1024);
      dma16(vw0, wsrc + pi * 81920, dst + 8192 + wave * 1024);
      if (wave >= 4) dma16_half(vw1, wsrc + pi * 81920, dst + 16384 + (wave & 3) * 512);
      pi = pi == 9 ? 0 : pi + 1;
    }
  }
  if (__builtin_expect(wave >= 8, 1)) {
    const unsigned tag = *(const __attribute__((address_space(4))) unsigned*)(p.ws + WS_EPOCH) + 1u;
    role_g(p, lds, wave, ti, tj, fI, fc, tag);
    asm volatile("s_endpgm");
    __builtin_unreachable();
  }
  int lane_a;
  asm volatile("v_mbcnt_lo_u32_b32 %0, -1, 0\n\tv_mbcnt_hi_u32_b32 %0, -1, %0" : "=v"(lane_a));
  role_a(p, lds, wave, lane_a, ti, tj);
}

extern "C" void kernel_launch(void* const* d_in, const int* in_sizes, int n_in, void* d_out, int out_size, void* d_ws,
                              size_t ws_size, hipStream_t stream) {
  Params p;
  p.x = (const char*)d_in[0]; p.wq = (const char*)d_in[1]; p.wk = (const char*)d_in[2]; p.wv = (const char*)d_in[3];
  p.wo = (const char*)d_in[4]; p.wg = (const char*)d_in[6]; p.wu = (const char*)d_in[7]; p.wd = (const char*)d_in[8];
  p.gw = (const char*)d_in[9]; p.out = (char*)d_out; p.ws = (char*)d_ws;
  (void)hipFuncSetAttribute((const void*)gated_mix_fused, hipFuncAttributeMaxDynamicSharedMemorySize, LDS_BYTES);
  hipLaunchKernelGGL(gated_mix_fused, dim3(256), dim3(768), LDS_BYTES, stream, p);
}
```
